```python
import torch

def _softmax(x, axis=-1):
    return torch.softmax(x, dim=axis)
def _gelu(x):
    return torch.nn.functional.gelu(x, approximate="tanh")
def _silu(x):
    return torch.nn.functional.silu(x)
def _sigmoid(x):
    return torch.sigmoid(x)
def _layernorm(x, axis=-1, eps=1e-5):
    m = x.mean(dim=axis, keepdim=True)
    v = x.var(dim=axis, keepdim=True, unbiased=False)
    return (x - m) / torch.sqrt(v + eps)
def _scatter_add(tgt, idx, src, axis=0):
    return tgt.clone().index_add_(axis, idx, src)
def _segment_sum(src, seg, n):
    out = torch.zeros((n,) + src.shape[1:], dtype=src.dtype)
    return out.index_add_(0, seg, src)
def _pad(x, pw):
    flat = tuple(v for pair in reversed(pw) for v in pair)
    return torch.nn.functional.pad(x, flat)

kernel_name = 'gated_mix__ssm_conv_s4__linear_attn_block__prompt'

def setup_inputs():

    _w = (lambda a: (lambda t: t.to(torch.bfloat16) if t.is_floating_point() else t)(torch.as_tensor(a)))
    def _A_setup():
        B, L, D, N = 1, 2048, 1024, 128
        A_re = -(lambda _x: torch.exp(torch.as_tensor(_x)))((lambda *s: torch.randn(*s, dtype=torch.bfloat16) * 3.2)(D, N))
        A_im = torch.pi * (lambda *s: torch.randn(*s, dtype=torch.bfloat16) * 0.05)(D, N)
        B_re = (lambda *s: torch.randn(*s, dtype=torch.bfloat16) * 0.2)(D, N)
        B_im = (lambda *s: torch.randn(*s, dtype=torch.bfloat16) * 0.2)(D, N)
        C_re = (lambda *s: torch.randn(*s, dtype=torch.bfloat16) * 0.2)(D, N)
        C_im = (lambda *s: torch.randn(*s, dtype=torch.bfloat16) * 0.2)(D, N)
        log_dt = (lambda *s: torch.randn(*s, dtype=torch.bfloat16) * 0.05)(D) * 0.5 - 4.0
        Dskip = (lambda *s: torch.randn(*s, dtype=torch.bfloat16) * 0.1)(D)
        return {
            "u": _w((lambda *s: torch.randn(*s, dtype=torch.bfloat16) * 0.05)(B, L, D)),
            "A_re": _w(A_re),
            "A_im": _w(A_im),
            "B_re": _w(B_re),
            "B_im": _w(B_im),
            "C_re": _w(C_re),
            "C_im": _w(C_im),
            "log_dt": _w(log_dt),
            "Dskip": _w(Dskip),
        }
    _A_in = _A_setup()

    def _B_setup():
        B, S, D = 1, 2048, 1024
        return {
            "x": _w((lambda *s: torch.randn(*s, dtype=torch.bfloat16) * 0.05)(B, S, D)),
            "wqkv": _w((lambda *s: torch.randn(*s, dtype=torch.bfloat16) * 0.8)(D, 3 * D)),
            "wo": _w((lambda *s: torch.randn(*s, dtype=torch.bfloat16) * 0.05)(D, D)),
        }
    _B_in = _B_setup()

    return {
        "A_u": _A_in["u"],
        "A_A_re": _A_in["A_re"],
        "A_A_im": _A_in["A_im"],
        "A_B_re": _A_in["B_re"],
        "A_B_im": _A_in["B_im"],
        "A_C_re": _A_in["C_re"],
        "A_C_im": _A_in["C_im"],
        "A_log_dt": _A_in["log_dt"],
        "A_Dskip": _A_in["Dskip"],
        "B_x": _B_in["x"],
        "B_wqkv": _B_in["wqkv"],
        "B_wo": _B_in["wo"],
        "gate_w": _w((lambda *s: torch.randn(*s, dtype=torch.bfloat16) * 0.05)(1024) * 16.0),
    }

def reference(A_u, A_A_re, A_A_im, A_B_re, A_B_im, A_C_re, A_C_im, A_log_dt, A_Dskip, B_x, B_wqkv, B_wo, gate_w):
    def _A_ref(u, A_re, A_im, B_re, B_im, C_re, C_im, log_dt, Dskip):
        Bsz, L, D = u.shape
        dt = torch.exp(log_dt)[:, None]
        A = A_re + 1j * A_im
        Bc = B_re + 1j * B_im
        Cc = C_re + 1j * C_im
        logdA = dt * A
        dB = dt * Bc
        l = (lambda x, dt: x.to(dt))(torch.arange(L), A_re.dtype)
        P = torch.exp(logdA[:, :, None] * l[None, None, :])
        K = torch.einsum("dn,dnl,dn->dl", Cc, P, dB).real
        L2 = 2 * L
        Kp = _pad(K, ((0, 0), (0, L)))
        up = (lambda x, *d: x.permute(*d))(u, 0, 2, 1)
        up = _pad(up, ((0, 0), (0, 0), (0, L)))
        Kf = (lambda x, axes=(-2,-1): torch.fft.rfft2(x.float(), dim=axes))(Kp[:, None, :], axes=(-2, -1))
        Uf = (lambda x, axes=(-2,-1): torch.fft.rfft2(x.float(), dim=axes))(up[:, :, None, :], axes=(-2, -1))
        Yf = Uf * Kf[None, :, :, :]
        y = (lambda x, s, axes=(-2,-1): torch.fft.irfft2(x, s=s, dim=axes).to(torch.bfloat16))(Yf, (1, L2), axes=(-2, -1))[:, :, 0, :L]
        y = (lambda x, *d: x.permute(*d))(y, 0, 2, 1) + u * Dskip[None, None, :]
        return y

    def _B_ref(x, wqkv, wo):
        B, S, D = x.shape
        H = 32; Dh = D // H
        qkv = (lambda x, *d: x.permute(*d))((x @ wqkv).reshape(B, S, 3, H, Dh), 2, 0, 3, 1, 4)
        q, k, v = qkv[0], qkv[1], qkv[2]
        phi = lambda t: torch.where(t > 0, t + 1, torch.exp(t)) + 1e-6
        pq, pk = phi(q), phi(k)
        kv = torch.einsum("bhsd,bhse->bhde", pk, v)
        z = torch.einsum("bhsd->bhd", pk)
        num = torch.einsum("bhqd,bhde->bhqe", pq, kv)
        den = torch.einsum("bhqd,bhd->bhq", pq, z)[..., None] + 1e-6
        o = num / den
        return (lambda x, *d: x.permute(*d))(o, 0, 2, 1, 3).reshape(B, S, D) @ wo

    x = A_u
    g = _sigmoid((x * gate_w).sum(-1, keepdims=True))
    a = _A_ref(x, A_A_re, A_A_im, A_B_re, A_B_im, A_C_re, A_C_im, A_log_dt, A_Dskip)
    b = _B_ref(x, B_wqkv, B_wo)
    return g * a + (1 - g) * b
```

```cpp
#include <hip/hip_runtime.h>
#include <hip/hip_bf16.h>
#include <stdint.h>

typedef __attribute__((ext_vector_type(8))) __bf16 bf16x8;
typedef __attribute__((ext_vector_type(8))) _Float16 half8;
typedef __attribute__((ext_vector_type(4))) _Float16 half4;
typedef __attribute__((ext_vector_type(4))) short s16x4;
typedef __attribute__((ext_vector_type(8))) short s16x8;
typedef __attribute__((ext_vector_type(16))) float f32x16;
typedef __attribute__((ext_vector_type(4))) float f32x4;
typedef __attribute__((ext_vector_type(4))) unsigned u32x4;
typedef __attribute__((ext_vector_type(2))) unsigned u32x2;
typedef unsigned short u16;

#define DI __device__ __forceinline__
#define LDS_AS __attribute__((address_space(3)))

constexpr int L_ = 2048, D_ = 1024, NST = 128, H_ = 32;

DI float bf2f(u16 x) { return __uint_as_float(((unsigned)x) << 16); }
DI u16 f2bf(float x) { unsigned u = __float_as_uint(x); u += 0x7fffu + ((u >> 16) & 1u); return (u16)(u >> 16); }
DI float bfr(float x) { unsigned u = __float_as_uint(x); u += 0x7fffu + ((u >> 16) & 1u); return __uint_as_float(u & 0xffff0000u); }
DI unsigned pack2bf(float lo, float hi) { return (unsigned)f2bf(lo) | ((unsigned)f2bf(hi) << 16); }

DI void glds16(const void* g, char* l) {
  __builtin_amdgcn_global_load_lds((const __attribute__((address_space(1))) void*)g, (LDS_AS void*)l, 16, 0, 0);
}
DI s16x4 tr_read(const char* p) {
  return __builtin_amdgcn_ds_read_tr16_b64_v4i16((LDS_AS s16x4*)(p));
}
DI bf16x8 cat_bf(s16x4 a, s16x4 b) {
  s16x8 r = __builtin_shufflevector(a, b, 0, 1, 2, 3, 4, 5, 6, 7);
  return __builtin_bit_cast(bf16x8, r);
}
#define MFMA_BF(a, b, c) __builtin_amdgcn_mfma_f32_32x32x16_bf16((a), (b), (c), 0, 0, 0)
#define MFMA_H(a, b, c) __builtin_amdgcn_mfma_f32_32x32x16_f16((a), (b), (c), 0, 0, 0)

DI float phi_f(float acc) {
  float t = bfr(acc);
  float r = t > 0.f ? bfr(t + 1.0f) : bfr(__expf(t));
  return bfr(r + 1e-6f);
}

constexpr size_t WS_PQ = 0;
constexpr size_t WS_O = WS_PQ + (size_t)L_ * D_ * 2;
constexpr size_t WS_YT = WS_O + (size_t)L_ * D_ * 2;
constexpr size_t WS_KVP = WS_YT + (size_t)L_ * D_ * 2;
constexpr size_t WS_ZP = WS_KVP + (size_t)16 * 32 * 1024 * 4;
constexpr size_t WS_G = WS_ZP + (size_t)16 * 32 * 32 * 4;

constexpr int A_LDS = 80 * 1024;

DI void gemm1_block(const u16* __restrict__ X, const u16* __restrict__ W, u16* __restrict__ pq, float* __restrict__ kvp,
                    float* __restrict__ zp, char* smem, int b) {
  const int xcd = b & 7, idx = b >> 3;
  const int rb = (xcd >> 1) * 4 + (idx & 3);
  const int hp = (xcd & 1) * 8 + (idx >> 2);
  const int tid = threadIdx.x, lane = tid & 63;
  const int wave = __builtin_amdgcn_readfirstlane(tid >> 6);
  const int wm = wave & 1, hh = wave >> 1;
  const int l31 = lane & 31, h = lane >> 5;
  const u16* ga[4];
  const u16* gb[6];
#pragma unroll
  for (int i = 0; i < 4; ++i) {
    int row = 32 * wave + 8 * i + (lane >> 3);
    int chunk = (lane & 7) ^ ((row >> 1) & 7);
    ga[i] = X + (size_t)(rb * 128 + row) * D_ + chunk * 8;
  }
#pragma unroll
  for (int i = 0; i < 6; ++i) {
    int bi = 6 * wave + i, slab = bi >> 3, part = bi & 7;
    int kr = 8 * part + (lane >> 3);
    int chunk = (lane & 7) ^ (((kr >> 1) & 1) << 2);
    gb[i] = W + (size_t)kr * 3072 + slab * 1024 + hp * 64 + chunk * 8;
  }
  auto stage = [&](int kt, int buf) {
    char* base = smem + buf * 40960;
#pragma unroll
    for (int i = 0; i < 4; ++i) glds16(ga[i] + kt * 64, base + (32 * wave + 8 * i) * 128);
#pragma unroll
    for (int i = 0; i < 6; ++i) glds16(gb[i] + (size_t)kt * 64 * 3072, base + 16384 + (6 * wave + i) * 1024);
  };
  int aoff[2][4];
#pragma unroll
  for (int rt = 0; rt < 2; ++rt) {
    int row = 64 * wm + 32 * rt + l31;
#pragma unroll
    for (int ks = 0; ks < 4; ++ks) aoff[rt][ks] = row * 128 + (((2 * ks + h) ^ ((row >> 1) & 7)) << 4);
  }
  const int blk = (lane >> 4) & 1, q4 = (lane & 15) >> 2, p4 = lane & 3;
  const int boff = 16384 + (8 * h + q4) * 128 + ((hh ^ (q4 >> 1)) << 6) + blk * 32 + p4 * 8;

  f32x16 accq[2], acck[2], accv[2];
#pragma unroll
  for (int i = 0; i < 16; ++i) { accq[0][i] = accq[1][i] = acck[0][i] = acck[1][i] = accv[0][i] = accv[1][i] = 0.f; }

  stage(0, 0);
#pragma unroll 1
  for (int kt = 0; kt < 16; ++kt) {
    asm volatile("s_waitcnt vmcnt(0)" ::: "memory");
    __syncthreads();
    if (kt + 1 < 16) stage(kt + 1, (kt + 1) & 1);
    const char* base = smem + (kt & 1) * 40960;
#pragma unroll
    for (int ks = 0; ks < 4; ++ks) {
      bf16x8 a0 = *(const bf16x8*)(base + aoff[0][ks]);
      bf16x8 a1 = *(const bf16x8*)(base + aoff[1][ks]);
      const char* bp = base + boff + ks * 2048;
      bf16x8 bq = cat_bf(tr_read(bp), tr_read(bp + 512));
      bf16x8 bk = cat_bf(tr_read(bp + 8192), tr_read(bp + 8192 + 512));
      bf16x8 bv = cat_bf(tr_read(bp + 16384), tr_read(bp + 16384 + 512));
      accq[0] = MFMA_BF(bq, a0, accq[0]);
      accq[1] = MFMA_BF(bq, a1, accq[1]);
      acck[0] = MFMA_BF(a0, bk, acck[0]);
      acck[1] = MFMA_BF(a1, bk, acck[1]);
      accv[0] = MFMA_BF(a0, bv, accv[0]);
      accv[1] = MFMA_BF(a1, bv, accv[1]);
    }
  }
  const int head = hp * 2 + hh;
#pragma unroll
  for (int rt = 0; rt < 2; ++rt) {
    int s = rb * 128 + 64 * wm + 32 * rt + l31;
#pragma unroll
    for (int g = 0; g < 4; ++g) {
      u32x2 v;
      v[0] = pack2bf(phi_f(accq[rt][4 * g]), phi_f(accq[rt][4 * g + 1]));
      v[1] = pack2bf(phi_f(accq[rt][4 * g + 2]), phi_f(accq[rt][4 * g + 3]));
      *(u32x2*)(pq + (size_t)s * D_ + head * 32 + 8 * g + 4 * h) = v;
    }
  }
  f32x16 kvacc;
#pragma unroll
  for (int i = 0; i < 16; ++i) kvacc[i] = 0.f;
  float zsum = 0.f;
#pragma unroll
  for (int rt = 0; rt < 2; ++rt) {
#pragma unroll
    for (int s = 0; s < 2; ++s) {
      u32x4 pa, pb;
#pragma unroll
      for (int j = 0; j < 4; ++j) {
        float k0 = phi_f(acck[rt][8 * s + 2 * j]), k1 = phi_f(acck[rt][8 * s + 2 * j + 1]);
        zsum += k0 + k1;
        pa[j] = pack2bf(k0, k1);
        pb[j] = pack2bf(accv[rt][8 * s + 2 * j], accv[rt][8 * s + 2 * j + 1]);
      }
      kvacc = MFMA_BF(__builtin_bit_cast(bf16x8, pa), __builtin_bit_cast(bf16x8, pb), kvacc);
    }
  }
  zsum += __shfl_xor(zsum, 32);
  __syncthreads();
  float* red = (float*)smem + hh * 1088;
  if (wm == 1) {
#pragma unroll
    for (int g = 0; g < 4; ++g) {
      f32x4 v = {kvacc[4 * g], kvacc[4 * g + 1], kvacc[4 * g + 2], kvacc[4 * g + 3]};
      *(f32x4*)(red + g * 256 + lane * 4) = v;
    }
    if (lane < 32) red[1024 + lane] = zsum;
  }
  __syncthreads();
  if (wm == 0) {
    float* dst = kvp + ((size_t)(rb * 32 + head) * 32 + l31) * 32 + 4 * h;
#pragma unroll
    for (int g = 0; g < 4; ++g) {
      f32x4 v = *(const f32x4*)(red + g * 256 + lane * 4);
      v[0] += kvacc[4 * g]; v[1] += kvacc[4 * g + 1]; v[2] += kvacc[4 * g + 2]; v[3] += kvacc[4 * g + 3];
      *(f32x4*)(dst + 8 * g) = v;
    }
    if (lane < 32) zp[(rb * 32 + head) * 32 + lane] = zsum + red[1024 + lane];
  }
}

constexpr int CV_GRID = 0;
constexpr int CV_COPY = 4160;
constexpr int CV_UIMG = CV_COPY + 8448;
constexpr int CV_WAVE = CV_UIMG + 6080 + 32;
constexpr int CV_SH = 4 * CV_WAVE;

DI float wave_max(float v) {
#pragma unroll
  for (int o = 32; o > 0; o >>= 1) v = fmaxf(v, __shfl_xor(v, o));
  return v;
}
DI float wave_sum(float v) {
#pragma unroll
  for (int o = 32; o > 0; o >>= 1) v += __shfl_xor(v, o);
  return v;
}
DI float pow2_scale(float m) {
  int e = (int)((__float_as_uint(m) >> 23) & 0xff);
  int se = 127 + 13 - (e - 127);
  se = se < 1 ? 1 : (se > 254 ? 254 : se);
  return __uint_as_float((unsigned)se << 23);
}
DI float sin_rev(float x) { return __builtin_amdgcn_sinf(__builtin_amdgcn_fractf(x)); }
DI float cos_rev(float x) { return __builtin_amdgcn_cosf(__builtin_amdgcn_fractf(x)); }

DI void conv_block(const u16* __restrict__ U, const u16* __restrict__ Are, const u16* __restrict__ Aim,
                   const u16* __restrict__ Bre, const u16* __restrict__ Bim, const u16* __restrict__ Cre,
                   const u16* __restrict__ Cim, const u16* __restrict__ logdt, u16* __restrict__ yT, char* smem, int cb) {
  const int tid = threadIdx.x, lane = tid & 63;
  const int wave = __builtin_amdgcn_readfirstlane(tid >> 6);
  const int d0 = cb * 4, d = d0 + wave;
  const int l31 = lane & 31, h = lane >> 5;
  char* wb = smem + wave * CV_WAVE;
  float* shmax = (float*)(smem + CV_SH);
  u32x2 uv[8];
#pragma unroll
  for (int i = 0; i < 8; ++i) uv[i] = *(const u32x2*)(U + (size_t)(tid + 256 * i) * D_ + d0);
  const float dt = bfr(__expf(bf2f(logdt[d])));
  float pa[2], pb[2], pwr[2], pwi[2], wmax = 0.f;
#pragma unroll
  for (int i = 0; i < 2; ++i) {
    int n = lane + 64 * i;
    float are = bf2f(Are[d * NST + n]), aim = bf2f(Aim[d * NST + n]);
    float bre = dt * bf2f(Bre[d * NST + n]), bim = dt * bf2f(Bim[d * NST + n]);
    float cre = bf2f(Cre[d * NST + n]), cim = bf2f(Cim[d * NST + n]);
    pa[i] = dt * are * 1.44269504088896f;
    pb[i] = dt * aim * 0.159154943091895f;
    pwr[i] = cre * bre - cim * bim;
    pwi[i] = cre * bim + cim * bre;
    wmax = fmaxf(wmax, fmaxf(fabsf(pwr[i]), fabsf(pwi[i])));
  }
  wmax = wave_max(wmax);
  const float wsc = wmax > 0.f ? pow2_scale(wmax) : 1.f;
  float k2048 = 0.f;
  float* par = (float*)(wb + CV_COPY);
#pragma unroll
  for (int i = 0; i < 2; ++i) {
    int n = lane + 64 * i;
    float wr = pwr[i] * wsc, wi = pwi[i] * wsc;
    float e1 = exp2f(pa[i] * 1024.f), c1 = cos_rev(pb[i] * 1024.f), s1 = sin_rev(pb[i] * 1024.f);
    float er = e1 * c1, ei = e1 * s1;
    float e2r = er * er - ei * ei, e2i = 2.f * er * ei;
    k2048 += wr * e2r - wi * e2i;
    f32x4 v0 = {pa[i], pb[i], wr, wi};
    f32x4 v1 = {er, ei, 0.f, 0.f};
    *(f32x4*)(par + n * 8) = v0;
    *(f32x4*)(par + n * 8 + 4) = v1;
  }
  k2048 = wave_sum(k2048);
  {
    unsigned mx[4] = {0, 0, 0, 0};
#pragma unroll
    for (int i = 0; i < 8; ++i) {
      mx[0] = max(mx[0], uv[i][0] & 0x7fffu); mx[1] = max(mx[1], (uv[i][0] >> 16) & 0x7fffu);
      mx[2] = max(mx[2], uv[i][1] & 0x7fffu); mx[3] = max(mx[3], (uv[i][1] >> 16) & 0x7fffu);
    }
#pragma unroll
    for (int c = 0; c < 4; ++c) {
      float m = wave_max(bf2f((u16)mx[c]));
      if (lane == 0) shmax[wave * 4 + c] = m;
    }
  }
  __syncthreads();
  float usc[4];
#pragma unroll
  for (int c = 0; c < 4; ++c) {
    float m = fmaxf(fmaxf(shmax[c], shmax[4 + c]), fmaxf(shmax[8 + c], shmax[12 + c]));
    usc[c] = m > 0.f ? pow2_scale(m) : 1.f;
  }
#pragma unroll
  for (int i = 0; i < 8; ++i) {
    int t = tid + 256 * i;
    int row = (t >> 5) + 31, e = t & 31;
    int off = row * 64 + ((((e >> 3) ^ ((row >> 2) & 3))) << 4) + (e & 7) * 2;
#pragma unroll
    for (int c = 0; c < 4; ++c) {
      unsigned bits = (c & 1) ? (uv[i][c >> 1] >> 16) : (uv[i][c >> 1] & 0xffffu);
      float f = bf2f((u16)bits) * usc[c];
      *(_Float16*)(smem + c * CV_WAVE + CV_UIMG + off) = (_Float16)f;
    }
  }
  for (int i = lane; i < 31 * 4; i += 64) *(u32x4*)(wb + CV_UIMG + i * 16) = (u32x4){0, 0, 0, 0};
  const float my_usc = usc[wave];
  f32x16 ka0, ka1;
#pragma unroll
  for (int i = 0; i < 16; ++i) ka0[i] = ka1[i] = 0.f;
  const float rf = (float)(32 * l31), cf = (float)l31;
#pragma unroll 1
  for (int t = 0; t < 16; ++t) {
    half8 fa0, fa1, fb;
#pragma unroll
    for (int jj = 0; jj < 4; ++jj) {
      int n = 8 * t + 4 * h + jj;
      f32x4 v0 = *(const f32x4*)(par + n * 8);
      float er = par[n * 8 + 4], ei = par[n * 8 + 5];
      float ex = exp2f(v0[0] * rf), ph = v0[1] * rf;
      float cx = ex * cos_rev(ph), sx = ex * sin_rev(ph);
      float xr = v0[2] * cx - v0[3] * sx, xi = v0[2] * sx + v0[3] * cx;
      float x1r = xr * er - xi * ei, x1i = xr * ei + xi * er;
      float ey = exp2f(v0[0] * cf), py = v0[1] * cf;
      float yr = ey * cos_rev(py), yi = ey * sin_rev(py);
      fa0[2 * jj] = (_Float16)xr; fa0[2 * jj + 1] = (_Float16)(-xi);
      fa1[2 * jj] = (_Float16)x1r; fa1[2 * jj + 1] = (_Float16)(-x1i);
      fb[2 * jj] = (_Float16)yr; fb[2 * jj + 1] = (_Float16)yi;
    }
    ka0 = MFMA_H(fa0, fb, ka0);
    ka1 = MFMA_H(fa1, fb, ka1);
  }
  float kmax = fabsf(k2048);
#pragma unroll
  for (int i = 0; i < 16; ++i) kmax = fmaxf(kmax, fmaxf(fabsf(ka0[i]), fabsf(ka1[i])));
  kmax = wave_max(kmax);
  const float ksc = kmax > 0.f ? pow2_scale(kmax) : 1.f;
  _Float16* grid = (_Float16*)(wb + CV_GRID);
#pragma unroll
  for (int i = 0; i < 16; ++i) {
    int r = (i & 3) + 8 * (i >> 2) + 4 * h;
    grid[32 * r + l31] = (_Float16)(ka0[i] * ksc);
    grid[1024 + 32 * r + l31] = (_Float16)(ka1[i] * ksc);
  }
  if (lane == 0) grid[2048] = (_Float16)(k2048 * ksc);
  __syncthreads();
  f32x16 y0, y1;
#pragma unroll
  for (int i = 0; i < 16; ++i) y0[i] = y1[i] = 0.f;
  const char* uimg = wb + CV_UIMG;
#pragma unroll 1
  for (int ph = 0; ph < 2; ++ph) {
    const int P = 32 * (32 * ph + 31) + 31;
#pragma unroll 1
    for (int gidx = lane; gidx < 4 * 264; gidx += 64) {
      int rho = gidx / 264, G = gidx - rho * 264;
      half4 v;
#pragma unroll
      for (int e = 0; e < 4; ++e) {
        int lag = P - (4 * G + e + rho);
        int lb = (int)bfr((float)lag);
        v[e] = lag >= 0 ? grid[lb < 0 ? 0 : lb] : (_Float16)0.f;
      }
      *(half4*)(wb + CV_COPY + rho * 2112 + G * 8) = v;
    }
    __builtin_amdgcn_wave_barrier();
    asm volatile("s_waitcnt lgkmcnt(0)" ::: "memory");
#pragma unroll 4
    for (int mm = 0; mm < 32; ++mm) {
      const int m = 32 * ph + mm;
#pragma unroll
      for (int kk = 0; kk < 2; ++kk) {
        int start = P - 32 * m + 16 * kk + 8 * h - l31;
        int rho = start & 3;
        const char* ap = wb + CV_COPY + rho * 2112 + (start - rho) * 2;
        half4 alo = *(const half4*)ap, ahi = *(const half4*)(ap + 8);
        half8 af = __builtin_shufflevector(alo, ahi, 0, 1, 2, 3, 4, 5, 6, 7);
        int ch = 2 * kk + h;
        {
          int row = 32 + l31 - m + 31;
          half8 bf = *(const half8*)(uimg + row * 64 + ((ch ^ ((row >> 2) & 3)) << 4));
          y1 = MFMA_H(af, bf, y1);
        }
        if (ph == 0) {
          int row = l31 - m + 31;
          half8 bf = *(const half8*)(uimg + row * 64 + ((ch ^ ((row >> 2) & 3)) << 4));
          y0 = MFMA_H(af, bf, y0);
        }
      }
    }
    __builtin_amdgcn_wave_barrier();
    asm volatile("s_waitcnt lgkmcnt(0)" ::: "memory");
  }
  const float osc = 1.0f / (wsc * ksc) / my_usc;
  u16* yd = yT + (size_t)d * L_;
#pragma unroll
  for (int g = 0; g < 4; ++g) {
    u32x2 v;
    v[0] = pack2bf(y0[4 * g] * osc, y0[4 * g + 1] * osc);
    v[1] = pack2bf(y0[4 * g + 2] * osc, y0[4 * g + 3] * osc);
    *(u32x2*)(yd + 32 * l31 + 8 * g + 4 * h) = v;
    v[0] = pack2bf(y1[4 * g] * osc, y1[4 * g + 1] * osc);
    v[1] = pack2bf(y1[4 * g + 2] * osc, y1[4 * g + 3] * osc);
    *(u32x2*)(yd + 1024 + 32 * l31 + 8 * g + 4 * h) = v;
  }
}

__global__ __launch_bounds__(256, 2) void kernelA(const u16* __restrict__ U, const u16* __restrict__ Are,
    const u16* __restrict__ Aim, const u16* __restrict__ Bre, const u16* __restrict__ Bim, const u16* __restrict__ Cre,
    const u16* __restrict__ Cim, const u16* __restrict__ logdt, const u16* __restrict__ Wqkv, char* __restrict__ ws) {
  extern __shared__ __attribute__((aligned(16))) char smem[];
  const int b = blockIdx.x;
  if (b < 256)
    gemm1_block(U, Wqkv, (u16*)(ws + WS_PQ), (float*)(ws + WS_KVP), (float*)(ws + WS_ZP), smem, b);
  else
    conv_block(U, Are, Aim, Bre, Bim, Cre, Cim, logdt, (u16*)(ws + WS_YT), smem, b - 256);
}

__global__ __launch_bounds__(256) void kernelO(const u16* __restrict__ U, const u16* __restrict__ gw, char* __restrict__ ws) {
  __shared__ __attribute__((aligned(16))) u16 kvT[32 * 40];
  __shared__ float zs[32];
  const int tid = threadIdx.x, lane = tid & 63, wave = tid >> 6, b = blockIdx.x;
  if (b >= 256) {
    float* G = (float*)(ws + WS_G);
    u32x4 w0 = *(const u32x4*)(gw + lane * 16), w1 = *(const u32x4*)(gw + lane * 16 + 8);
    for (int i = 0; i < 8; ++i) {
      int row = (b - 256) * 32 + wave * 8 + i;
      u32x4 x0 = *(const u32x4*)(U + (size_t)row * D_ + lane * 16), x1 = *(const u32x4*)(U + (size_t)row * D_ + lane * 16 + 8);
      float s = 0.f;
#pragma unroll
      for (int j = 0; j < 4; ++j) {
        s += bfr(bf2f((u16)(x0[j] & 0xffff)) * bf2f((u16)(w0[j] & 0xffff)));
        s += bfr(bf2f((u16)(x0[j] >> 16)) * bf2f((u16)(w0[j] >> 16)));
        s += bfr(bf2f((u16)(x1[j] & 0xffff)) * bf2f((u16)(w1[j] & 0xffff)));
        s += bfr(bf2f((u16)(x1[j] >> 16)) * bf2f((u16)(w1[j] >> 16)));
      }
      s = bfr(wave_sum(s));
      float g = bfr(1.0f / (1.0f + __expf(-s)));
      if (lane == 0) G[row] = g;
    }
    return;
  }
  const int head = b >> 3, chunk = b & 7;
  const float* kvp = (const float*)(ws + WS_KVP);
  const float* zp = (const float*)(ws + WS_ZP);
  {
    int e = tid >> 3, d4 = (tid & 7) * 4;
    f32x4 s = {0.f, 0.f, 0.f, 0.f};
#pragma unroll
    for (int r = 0; r < 16; ++r) s += *(const f32x4*)(kvp + ((size_t)(r * 32 + head) * 32 + e) * 32 + d4);
    u32x2 v;
    v[0] = pack2bf(s[0], s[1]); v[1] = pack2bf(s[2], s[3]);
    *(u32x2*)(kvT + e * 40 + d4) = v;
    if (tid < 32) {
      float z = 0.f;
#pragma unroll
      for (int r = 0; r < 16; ++r) z += zp[(r * 32 + head) * 32 + tid];
      zs[tid] = bfr(z);
    }
  }
  __syncthreads();
  const int l31 = lane & 31, h = lane >> 5;
  const u16* pq = (const u16*)(ws + WS_PQ);
  u16* o = (u16*)(ws + WS_O);
  bf16x8 ka[2];
  float zr[2][8];
#pragma unroll
  for (int ks = 0; ks < 2; ++ks) {
    ka[ks] = *(const bf16x8*)(kvT + l31 * 40 + 16 * ks + 8 * h);
#pragma unroll
    for (int j = 0; j < 8; ++j) zr[ks][j] = zs[16 * ks + 8 * h + j];
  }
#pragma unroll
  for (int rt = 0; rt < 2; ++rt) {
    int s = chunk * 256 + wave * 64 + rt * 32 + l31;
    f32x16 acc;
#pragma unroll
    for (int i = 0; i < 16; ++i) acc[i] = 0.f;
    float den = 0.f;
#pragma unroll
    for (int ks = 0; ks < 2; ++ks) {
      u32x4 qv = *(const u32x4*)(pq + (size_t)s * D_ + head * 32 + 16 * ks + 8 * h);
#pragma unroll
      for (int j = 0; j < 4; ++j) {
        den += bf2f((u16)(qv[j] & 0xffff)) * zr[ks][2 * j];
        den += bf2f((u16)(qv[j] >> 16)) * zr[ks][2 * j + 1];
      }
      acc = MFMA_BF(ka[ks], __builtin_bit_cast(bf16x8, qv), acc);
    }
    den += __shfl_xor(den, 32);
    den = bfr(bfr(den) + 1e-6f);
    float rd = 1.0f / den;
#pragma unroll
    for (int g = 0; g < 4; ++g) {
      u32x2 v;
      v[0] = pack2bf(bfr(acc[4 * g]) * rd, bfr(acc[4 * g + 1]) * rd);
      v[1] = pack2bf(bfr(acc[4 * g + 2]) * rd, bfr(acc[4 * g + 3]) * rd);
      *(u32x2*)(o + (size_t)s * D_ + head * 32 + 8 * g + 4 * h) = v;
    }
  }
}

constexpr int B_STAGE = 24576, B_NST = 3;
__global__ __launch_bounds__(256) void kernelB(const u16* __restrict__ U, const u16* __restrict__ Dskip,
                                                 const u16* __restrict__ Wo, const char* __restrict__ ws, u16* __restrict__ out) {
  extern __shared__ __attribute__((aligned(16))) char smem[];
  const int b = blockIdx.x, xcd = b & 7, idx = b >> 3;
  const int rb = (xcd >> 1) * 4 + (idx & 3);
  const int cbk = (xcd & 1) * 8 + (idx >> 2);
  const int tid = threadIdx.x, lane = tid & 63;
  const int wave = __builtin_amdgcn_readfirstlane(tid >> 6);
  const int wm = wave & 1, wn = wave >> 1;
  const int l31 = lane & 31, h = lane >> 5;
  const u16* O = (const u16*)(ws + WS_O);
  const u16* ga[4];
  const u16* gb[2];
#pragma unroll
  for (int i = 0; i < 4; ++i) {
    int row = 32 * wave + 8 * i + (lane >> 3);
    int chunk = (lane & 7) ^ ((row >> 1) & 7);
    ga[i] = O + (size_t)(rb * 128 + row) * D_ + chunk * 8;
  }
#pragma unroll
  for (int i = 0; i < 2; ++i) {
    int kr = 8 * (2 * wave + i) + (lane >> 3);
    int chunk = (lane & 7) ^ (((kr >> 1) & 1) << 2);
    gb[i] = Wo + (size_t)kr * D_ + cbk * 64 + chunk * 8;
  }
  auto stage = [&](int kt, int buf) {
    char* base = smem + buf * B_STAGE;
#pragma unroll
    for (int i = 0; i < 4; ++i) glds16(ga[i] + kt * 64, base + (32 * wave + 8 * i) * 128);
#pragma unroll
    for (int i = 0; i < 2; ++i) glds16(gb[i] + (size_t)kt * 64 * D_, base + 16384 + (2 * wave + i) * 1024);
  };
  int aoff[2][4];
#pragma unroll
  for (int rt = 0; rt < 2; ++rt) {
    int row = 64 * wm + 32 * rt + l31;
#pragma unroll
    for (int ks = 0; ks < 4; ++ks) aoff[rt][ks] = row * 128 + (((2 * ks + h) ^ ((row >> 1) & 7)) << 4);
  }
  const int blk = (lane >> 4) & 1, q4 = (lane & 15) >> 2, p4 = lane & 3;
  const int boff = 16384 + (8 * h + q4) * 128 + ((wn ^ (q4 >> 1)) << 6) + blk * 32 + p4 * 8;
  f32x16 acc[2];
#pragma unroll
  for (int i = 0; i < 16; ++i) acc[0][i] = acc[1][i] = 0.f;
  stage(0, 0);
  stage(1, 1);
  for (int kt = 0; kt < 16; ++kt) {
    if (kt + 1 < 16) asm volatile("s_waitcnt vmcnt(6)" ::: "memory");
    else asm volatile("s_waitcnt vmcnt(0)" ::: "memory");
    asm volatile("s_waitcnt lgkmcnt(0)" ::: "memory");
    __builtin_amdgcn_s_barrier();
    if (kt + 2 < 16) stage(kt + 2, (kt + 2) % 3);
    const char* base = smem + (kt % 3) * B_STAGE;
#pragma unroll
    for (int ks = 0; ks < 4; ++ks) {
      bf16x8 a0 = *(const bf16x8*)(base + aoff[0][ks]);
      bf16x8 a1 = *(const bf16x8*)(base + aoff[1][ks]);
      const char* bp = base + boff + ks * 2048;
      bf16x8 bb = cat_bf(tr_read(bp), tr_read(bp + 512));
      acc[0] = MFMA_BF(a0, bb, acc[0]);
      acc[1] = MFMA_BF(a1, bb, acc[1]);
    }
  }
  const int dcol = cbk * 64 + wn * 32 + l31;
  const float dsk = bf2f(Dskip[dcol]);
  const float* G = (const float*)(ws + WS_G);
  const u16* yT = (const u16*)(ws + WS_YT) + (size_t)dcol * L_;
#pragma unroll
  for (int rt = 0; rt < 2; ++rt) {
#pragma unroll
    for (int g = 0; g < 4; ++g) {
      int t0 = rb * 128 + 64 * wm + 32 * rt + 8 * g + 4 * h;
      u32x2 yv = *(const u32x2*)(yT + t0);
      f32x4 gv = *(const f32x4*)(G + t0);
#pragma unroll
      for (int e = 0; e < 4; ++e) {
        int t = t0 + e;
        float y = bf2f((u16)((e & 1) ? (yv[e >> 1] >> 16) : (yv[e >> 1] & 0xffff)));
        float u = bf2f(U[(size_t)t * D_ + dcol]);
        float a = bfr(y + bfr(u * dsk));
        float bb = bfr(acc[rt][4 * g + e]);
        float gg = gv[e];
        float r = bfr(gg * a) + bfr(bfr(1.0f - gg) * bb);
        out[(size_t)t * D_ + dcol] = f2bf(r);
      }
    }
  }
}

extern "C" void kernel_launch(void* const* d_in, const int* in_sizes, int n_in, void* d_out, int out_size, void* d_ws,
                              size_t ws_size, hipStream_t stream) {
  const u16* U = (const u16*)d_in[0];
  char* ws = (char*)d_ws;
  (void)hipFuncSetAttribute((const void*)kernelA, hipFuncAttributeMaxDynamicSharedMemorySize, A_LDS);
  (void)hipFuncSetAttribute((const void*)kernelB, hipFuncAttributeMaxDynamicSharedMemorySize, B_STAGE * B_NST);
  kernelA<<<512, 256, A_LDS, stream>>>(U, (const u16*)d_in[1], (const u16*)d_in[2], (const u16*)d_in[3],
                                       (const u16*)d_in[4], (const u16*)d_in[5], (const u16*)d_in[6],
                                       (const u16*)d_in[7], (const u16*)d_in[10], ws);
  kernelO<<<256 + 64, 256, 0, stream>>>(U, (const u16*)d_in[12], ws);
  kernelB<<<256, 256, B_STAGE * B_NST, stream>>>(U, (const u16*)d_in[8], (const u16*)d_in[11], ws, (u16*)d_out);
}
```

```cpp
#include <hip/hip_runtime.h>
#include <hip/hip_bf16.h>
#include <stdint.h>

typedef __attribute__((ext_vector_type(8))) __bf16 bf16x8;
typedef __attribute__((ext_vector_type(8))) _Float16 half8;
typedef __attribute__((ext_vector_type(4))) _Float16 half4;
typedef __attribute__((ext_vector_type(2))) _Float16 half2v;
typedef __attribute__((ext_vector_type(4))) short s16x4;
typedef __attribute__((ext_vector_type(8))) short s16x8;
typedef __attribute__((ext_vector_type(16))) float f32x16;
typedef __attribute__((ext_vector_type(4))) float f32x4;
typedef __attribute__((ext_vector_type(2))) float f32x2;
typedef __attribute__((ext_vector_type(4))) unsigned u32x4;
typedef __attribute__((ext_vector_type(2))) unsigned u32x2;
typedef unsigned short u16;

#define DI __device__ __forceinline__
#define LDS_AS __attribute__((address_space(3)))

constexpr int L_ = 2048, D_ = 1024, NST = 128;

DI float bf2f(u16 x) { return __uint_as_float(((unsigned)x) << 16); }
DI u16 f2bf(float x) { unsigned u = __float_as_uint(x); u += 0x7fffu + ((u >> 16) & 1u); return (u16)(u >> 16); }
DI float bfr(float x) { unsigned u = __float_as_uint(x); u += 0x7fffu + ((u >> 16) & 1u); return __uint_as_float(u & 0xffff0000u); }
typedef __attribute__((ext_vector_type(2))) __bf16 bf16x2v;
DI unsigned pack2bf(float lo, float hi) { f32x2 v = {lo, hi}; return __builtin_bit_cast(unsigned, __builtin_convertvector(v, bf16x2v)); }
DI float lo_f(unsigned p) { return __uint_as_float(p << 16); }
DI float hi_f(unsigned p) { return __uint_as_float(p & 0xffff0000u); }
DI unsigned pack2h(float lo, float hi) { f32x2 v = {lo, hi}; return __builtin_bit_cast(unsigned, __builtin_convertvector(v, half2v)); }

DI void glds16(const void* g, char* l) {
  __builtin_amdgcn_global_load_lds((const __attribute__((address_space(1))) void*)g, (LDS_AS void*)l, 16, 0, 0);
}
DI bf16x8 cat_bf(s16x4 a, s16x4 b) {
  s16x8 r = __builtin_shufflevector(a, b, 0, 1, 2, 3, 4, 5, 6, 7);
  return __builtin_bit_cast(bf16x8, r);
}
DI half8 cat_h(s16x4 a, s16x4 b) {
  s16x8 r = __builtin_shufflevector(a, b, 0, 1, 2, 3, 4, 5, 6, 7);
  return __builtin_bit_cast(half8, r);
}
DI unsigned lds_addr(const char* p) { return (unsigned)(size_t)(LDS_AS const char*)p; }
#define DS_B128(dst, addr, off) asm volatile("ds_read_b128 %0, %1 offset:%2" : "=v"(dst) : "v"(addr), "i"(off) : "memory")
#define DS_B64(dst, addr, off) asm volatile("ds_read_b64 %0, %1 offset:%2" : "=v"(dst) : "v"(addr), "i"(off) : "memory")
#define DS_TR(dst, addr, off) asm volatile("ds_read_b64_tr_b16 %0, %1 offset:%2" : "=v"(dst) : "v"(addr), "i"(off) : "memory")
#define LGKM(n) do { asm volatile("s_waitcnt lgkmcnt(" #n ")" ::: "memory"); __builtin_amdgcn_sched_barrier(0); } while (0)
#define SCHED() __builtin_amdgcn_sched_barrier(0)
#define MFMA_BF(a, b, c) __builtin_amdgcn_mfma_f32_32x32x16_bf16((a), (b), (c), 0, 0, 0)
#define MFMA_H(a, b, c) __builtin_amdgcn_mfma_f32_32x32x16_f16((a), (b), (c), 0, 0, 0)

DI unsigned phi2(float a0, float a1) {
  unsigned t = pack2bf(a0, a1);
  float t0 = lo_f(t), t1 = hi_f(t);
  float r0 = t0 > 0.f ? t0 + 1.0f : __builtin_amdgcn_exp2f(t0 * 1.44269504088896f);
  float r1 = t1 > 0.f ? t1 + 1.0f : __builtin_amdgcn_exp2f(t1 * 1.44269504088896f);
  unsigned r = pack2bf(r0, r1);
  return pack2bf(lo_f(r) + 1e-6f, hi_f(r) + 1e-6f);
}

#define STAMP(id)
#define CSTAMP(id)
constexpr size_t WS_PQ = 0;
constexpr size_t WS_O = WS_PQ + (size_t)L_ * D_ * 2;
constexpr size_t WS_YT = WS_O + (size_t)L_ * D_ * 2;
constexpr size_t WS_KVP = WS_YT + (size_t)L_ * D_ * 2;
constexpr size_t WS_ZP = WS_KVP + (size_t)16 * 32 * 1024 * 4;
constexpr size_t WS_G = WS_ZP + (size_t)16 * 32 * 32 * 4;
constexpr size_t WS_UT = WS_G + 8192;
constexpr size_t WS_KG = WS_UT + (size_t)L_ * D_ * 2;
constexpr size_t WS_KS = WS_KG + (size_t)D_ * 2080 * 2;
constexpr size_t WS_STAMP = 64u << 20;
constexpr int A_LDS = 80 * 1024;

constexpr int G1_STAGE = 20480;
DI void gemm1_block(const u16* __restrict__ X, const u16* __restrict__ W, u16* __restrict__ pq, float* __restrict__ kvp,
                    float* __restrict__ zp, char* smem, int b) {
  char* stamp_base = (char*)pq + WS_STAMP; (void)stamp_base;
  STAMP(0);
  const int xcd = b & 7, idx = b >> 3;
  const int rb = (xcd >> 2) * 8 + (idx & 7);
  const int hp = (xcd & 3) * 4 + (idx >> 3);
  const int tid = threadIdx.x, lane = tid & 63;
  const int wave = __builtin_amdgcn_readfirstlane(tid >> 6);
  const int wm = wave & 3, hh = wave >> 2;
  const int l31 = lane & 31, h = lane >> 5;
  const u16* ga;
  {
    int row = 16 * wave + (lane >> 2);
    int chunk = (lane & 3) ^ ((row >> 2) & 3);
    ga = X + (size_t)(rb * 128 + row) * D_ + chunk * 8;
  }
  const u16* gbl;
  {
    int kr = lane >> 3;
    int chunk = (lane & 7) ^ (((kr >> 1) & 1) << 2);
    gbl = W + (size_t)kr * 3072 + hp * 64 + chunk * 8;
  }
  auto stage = [&](int kt) {
    char* base = smem + (kt & 3) * G1_STAGE;
    glds16(ga + kt * 32, base + wave * 1024);
    int j0 = (wave + 4 * kt) & 7;
    {
      int slab = j0 >> 2, part = j0 & 3;
      glds16(gbl + (size_t)(kt * 32 + part * 8) * 3072 + slab * 1024, base + 8192 + j0 * 1024);
    }
    if (j0 < 4) glds16(gbl + (size_t)(kt * 32 + j0 * 8) * 3072 + 2048, base + 8192 + (8 + j0) * 1024);
  };
  const unsigned sb = lds_addr(smem);
  unsigned aad[2];
  {
    int row = 32 * wm + l31;
#pragma unroll
    for (int ks = 0; ks < 2; ++ks) aad[ks] = sb + row * 64 + (((2 * ks + h) ^ ((row >> 2) & 3)) << 4);
  }
  const int blk = (lane >> 4) & 1, q4 = (lane & 15) >> 2, p4 = lane & 3;
  const unsigned bad = sb + 8192 + (8 * h + q4) * 128 + ((hh ^ (q4 >> 1)) << 6) + blk * 32 + p4 * 8;

  f32x16 accq, acck, accv;
#pragma unroll
  for (int i = 0; i < 16; ++i) accq[i] = acck[i] = accv[i] = 0.f;

  const unsigned aadh[2] = {aad[0] + 2 * G1_STAGE, aad[1] + 2 * G1_STAGE};
  const unsigned badh = bad + 2 * G1_STAGE;
  bf16x8 a[2]; s16x4 bl[2][3], bh[2][3];
#define G1_RD2(ks, AA, BB, bo) \
  DS_B128(a[ks], AA[ks], bo); \
  DS_TR(bl[ks][0], BB, bo + ks * 2048); DS_TR(bh[ks][0], BB, bo + ks * 2048 + 512); \
  DS_TR(bl[ks][1], BB, bo + ks * 2048 + 4096); DS_TR(bh[ks][1], BB, bo + ks * 2048 + 4096 + 512); \
  DS_TR(bl[ks][2], BB, bo + ks * 2048 + 8192); DS_TR(bh[ks][2], BB, bo + ks * 2048 + 8192 + 512);
#define G1_RD(ks, buf) if constexpr ((buf) < 2) { G1_RD2(ks, aad, bad, (buf) * G1_STAGE) } else { G1_RD2(ks, aadh, badh, ((buf) - 2) * G1_STAGE) }
#define G1_MM(ks) { \
  accq = MFMA_BF(cat_bf(bl[ks][0], bh[ks][0]), a[ks], accq); \
  acck = MFMA_BF(a[ks], cat_bf(bl[ks][1], bh[ks][1]), acck); \
  accv = MFMA_BF(a[ks], cat_bf(bl[ks][2], bh[ks][2]), accv); }
#define G1_STEP(kt, buf, WAITN, MORE) { \
    G1_RD(1, buf) \
    LGKM(7); G1_MM(0) SCHED(); \
    asm volatile("s_waitcnt vmcnt(" #WAITN ") lgkmcnt(0)" ::: "memory"); \
    __builtin_amdgcn_s_barrier(); \
    if ((kt) + 4 < 32) stage((kt) + 4); \
    if (MORE) { G1_RD(0, ((buf) + 1) & 3) } \
    SCHED(); G1_MM(1) asm volatile("" : "+v"(accq), "+v"(acck), "+v"(accv)); SCHED(); }
  stage(0); stage(1); stage(2); stage(3);
  asm volatile("s_waitcnt vmcnt(7)" ::: "memory");
  __builtin_amdgcn_s_barrier();
  G1_RD(0, 0)
#pragma unroll 1
  for (int kt = 0; kt < 28; kt += 4) {
    G1_STEP(kt, 0, 5, 1) G1_STEP(kt + 1, 1, 5, 1) G1_STEP(kt + 2, 2, 5, 1) G1_STEP(kt + 3, 3, 5, 1)
  }
  G1_STEP(28, 0, 5, 1) G1_STEP(29, 1, 0, 1) G1_STEP(30, 2, 0, 1) G1_STEP(31, 3, 0, 0)
  STAMP(1);
  const int head = hp * 2 + hh;
  {
    int s = rb * 128 + 32 * wm + l31;
#pragma unroll
    for (int g = 0; g < 4; ++g) {
      u32x2 v;
      v[0] = phi2(accq[4 * g], accq[4 * g + 1]);
      v[1] = phi2(accq[4 * g + 2], accq[4 * g + 3]);
      *(u32x2*)(pq + (size_t)s * D_ + head * 32 + 8 * g + 4 * h) = v;
    }
  }
  f32x16 kvacc;
#pragma unroll
  for (int i = 0; i < 16; ++i) kvacc[i] = 0.f;
  float zsum = 0.f;
#pragma unroll
  for (int s = 0; s < 2; ++s) {
    u32x4 pa, pb;
#pragma unroll
    for (int j = 0; j < 4; ++j) {
      pa[j] = phi2(acck[8 * s + 2 * j], acck[8 * s + 2 * j + 1]);
      zsum += lo_f(pa[j]) + hi_f(pa[j]);
      pb[j] = pack2bf(accv[8 * s + 2 * j], accv[8 * s + 2 * j + 1]);
    }
    kvacc = MFMA_BF(__builtin_bit_cast(bf16x8, pa), __builtin_bit_cast(bf16x8, pb), kvacc);
  }
  zsum += __shfl_xor(zsum, 32);
  __syncthreads();
  float* red = (float*)smem + (hh * 3 + wm - 1) * 1088;
  if (wm != 0) {
#pragma unroll
    for (int g = 0; g < 4; ++g) {
      f32x4 v = {kvacc[4 * g], kvacc[4 * g + 1], kvacc[4 * g + 2], kvacc[4 * g + 3]};
      *(f32x4*)(red + g * 256 + lane * 4) = v;
    }
    if (lane < 32) red[1024 + lane] = zsum;
  }
  __syncthreads();
  if (wm == 0) {
    float* dst = kvp + ((size_t)(rb * 32 + head) * 32 + l31) * 32 + 4 * h;
    const float* r0 = (const float*)smem + hh * 3 * 1088;
#pragma unroll
    for (int g = 0; g < 4; ++g) {
      f32x4 v = {kvacc[4 * g], kvacc[4 * g + 1], kvacc[4 * g + 2], kvacc[4 * g + 3]};
#pragma unroll
      for (int w = 0; w < 3; ++w) v += *(const f32x4*)(r0 + w * 1088 + g * 256 + lane * 4);
      *(f32x4*)(dst + 8 * g) = v;
    }
    if (lane < 32) zp[(rb * 32 + head) * 32 + lane] = zsum + r0[1024 + lane] + r0[1088 + 1024 + lane] + r0[2 * 1088 + 1024 + lane];
  }
  STAMP(2);
}

DI float wave_max(float v) {
#pragma unroll
  for (int o = 32; o > 0; o >>= 1) v = fmaxf(v, __shfl_xor(v, o));
  return v;
}
DI float wave_sum(float v) {
#pragma unroll
  for (int o = 32; o > 0; o >>= 1) v += __shfl_xor(v, o);
  return v;
}
DI float pow2_scale(float m) {
  int e = (int)((__float_as_uint(m) >> 23) & 0xff);
  int se = 127 + 13 - (e - 127);
  se = se < 1 ? 1 : (se > 254 ? 254 : se);
  return __uint_as_float((unsigned)se << 23);
}
DI float ex2(float x) { return __builtin_amdgcn_exp2f(x); }
DI float sin_rev(float x) { return __builtin_amdgcn_sinf(__builtin_amdgcn_fractf(x)); }
DI float cos_rev(float x) { return __builtin_amdgcn_cosf(__builtin_amdgcn_fractf(x)); }

constexpr int AX_PAR = 0;
constexpr int AX_XCH = 4096;
constexpr int AX_CH = 12288;
constexpr int AX_TR = 4 * AX_CH;

DI void aux_block(const u16* __restrict__ U, const u16* __restrict__ Are, const u16* __restrict__ Aim,
                  const u16* __restrict__ Bre, const u16* __restrict__ Bim, const u16* __restrict__ Cre,
                  const u16* __restrict__ Cim, const u16* __restrict__ logdt, char* __restrict__ ws, char* smem, int b) {
  char* stamp_base = ws + WS_STAMP; (void)stamp_base;
  STAMP(0);
  const int tid = threadIdx.x, lane = tid & 63;
  const int wave = __builtin_amdgcn_readfirstlane(tid >> 6);
  const int ch = wave >> 1, hw = wave & 1;
  const int d = b * 4 + ch;
  const int l31 = lane & 31, h = lane >> 5;
  char* cbp = smem + ch * AX_CH;
  const int tr0 = (b >> 4) * 128, tc0 = (b & 15) * 64;
  u32x4 ut[2];
#pragma unroll
  for (int i = 0; i < 2; ++i) {
    int e = tid + 512 * i;
    ut[i] = *(const u32x4*)(U + (size_t)(tr0 + (e >> 3)) * D_ + tc0 + (e & 7) * 8);
  }
  const float dt = bfr(__expf(bf2f(logdt[d])));
  float pa[2], pb[2], pwr[2], pwi[2], wmax = 0.f;
#pragma unroll
  for (int i = 0; i < 2; ++i) {
    int n = lane + 64 * i;
    float are = bf2f(Are[d * NST + n]), aim = bf2f(Aim[d * NST + n]);
    float bre = dt * bf2f(Bre[d * NST + n]), bim = dt * bf2f(Bim[d * NST + n]);
    float cre = bf2f(Cre[d * NST + n]), cim = bf2f(Cim[d * NST + n]);
    pa[i] = dt * are * 1.44269504088896f;
    pb[i] = dt * aim * 0.159154943091895f;
    pwr[i] = cre * bre - cim * bim;
    pwi[i] = cre * bim + cim * bre;
    wmax = fmaxf(wmax, fmaxf(fabsf(pwr[i]), fabsf(pwi[i])));
  }
  wmax = wave_max(wmax);
  const float wsc = wmax > 0.f ? pow2_scale(wmax) : 1.f;
  float k2048 = 0.f;
  float* par = (float*)(cbp + AX_PAR);
#pragma unroll
  for (int i = 0; i < 2; ++i) {
    float wr = pwr[i] * wsc, wi = pwi[i] * wsc;
    float e1 = ex2(pa[i] * 1024.f), c1 = cos_rev(pb[i] * 1024.f), s1 = sin_rev(pb[i] * 1024.f);
    float er = e1 * c1, ei = e1 * s1;
    float e2r = er * er - ei * ei, e2i = 2.f * er * ei;
    k2048 += wr * e2r - wi * e2i;
    if (i == hw) {
      int n = lane + 64 * i;
      *(f32x4*)(par + n * 8) = (f32x4){pa[i], pb[i], wr, wi};
      *(f32x4*)(par + n * 8 + 4) = (f32x4){er, ei, 0.f, 0.f};
    }
  }
  k2048 = wave_sum(k2048);
#pragma unroll
  for (int i = 0; i < 2; ++i) {
    int e = tid + 512 * i, row = e >> 3, c8 = (e & 7) * 8;
#pragma unroll
    for (int j = 0; j < 4; ++j) {
      *(u16*)(smem + AX_TR + (c8 + 2 * j) * 272 + row * 2) = (u16)(ut[i][j] & 0xffffu);
      *(u16*)(smem + AX_TR + (c8 + 2 * j + 1) * 272 + row * 2) = (u16)(ut[i][j] >> 16);
    }
  }
  __syncthreads();
  {
    u16* uT = (u16*)(ws + WS_UT);
#pragma unroll
    for (int i = 0; i < 2; ++i) {
      int e = tid + 512 * i, c = e >> 4, t8 = (e & 15) * 8;
      u32x4 v = *(const u32x4*)(smem + AX_TR + c * 272 + t8 * 2);
      *(u32x4*)(uT + (size_t)(tc0 + c) * L_ + tr0 + t8) = v;
    }
  }
  STAMP(1); CSTAMP(40);
  f32x16 ka0, ka1;
#pragma unroll
  for (int i = 0; i < 16; ++i) ka0[i] = ka1[i] = 0.f;
  const float rf = (float)(32 * l31), cf = (float)l31;
  unsigned paddr = lds_addr(cbp + AX_PAR) + (64 * hw + 4 * h) * 32;
  f32x4 P0[2][4]; f32x2 P1[2][4];
#define KG_RD(S) { _Pragma("unroll") for (int jj = 0; jj < 4; ++jj) { DS_B128(P0[S][jj], paddr, jj * 32); DS_B64(P1[S][jj], paddr, jj * 32 + 16); } paddr += 256; }
#define KG_DO(S) { u32x4 fa0, fa1, fb; \
    _Pragma("unroll") for (int jj = 0; jj < 4; ++jj) { \
      f32x4 v0 = P0[S][jj]; f32x2 v1 = P1[S][jj]; \
      float ex = ex2(v0[0] * rf), ph = v0[1] * rf; \
      float cx = ex * cos_rev(ph), sx = ex * sin_rev(ph); \
      float xr = v0[2] * cx - v0[3] * sx, xi = v0[2] * sx + v0[3] * cx; \
      float x1r = xr * v1[0] - xi * v1[1], x1i = xr * v1[1] + xi * v1[0]; \
      float ey = ex2(v0[0] * cf), py = v0[1] * cf; \
      fa0[jj] = pack2h(xr, -xi); fa1[jj] = pack2h(x1r, -x1i); fb[jj] = pack2h(ey * cos_rev(py), ey * sin_rev(py)); } \
    ka0 = MFMA_H(__builtin_bit_cast(half8, fa0), __builtin_bit_cast(half8, fb), ka0); \
    ka1 = MFMA_H(__builtin_bit_cast(half8, fa1), __builtin_bit_cast(half8, fb), ka1); }
  asm volatile("s_waitcnt lgkmcnt(0)" ::: "memory");
  KG_RD(0)
#pragma unroll 1
  for (int t = 0; t < 8; t += 2) {
    KG_RD(1) LGKM(8); KG_DO(0) SCHED();
    if (t + 2 < 8) { KG_RD(0) LGKM(8); } else { LGKM(0); }
    KG_DO(1) SCHED();
  }
  STAMP(2); CSTAMP(41);
  float* xch = (float*)(cbp + AX_XCH);
  if (hw == 1) {
#pragma unroll
    for (int g = 0; g < 4; ++g) {
      *(f32x4*)(xch + g * 256 + lane * 4) = (f32x4){ka0[4 * g], ka0[4 * g + 1], ka0[4 * g + 2], ka0[4 * g + 3]};
      *(f32x4*)(xch + 1024 + g * 256 + lane * 4) = (f32x4){ka1[4 * g], ka1[4 * g + 1], ka1[4 * g + 2], ka1[4 * g + 3]};
    }
  }
  __syncthreads();
  if (hw == 0) {
#pragma unroll
    for (int g = 0; g < 4; ++g) {
      f32x4 v0 = *(const f32x4*)(xch + g * 256 + lane * 4), v1 = *(const f32x4*)(xch + 1024 + g * 256 + lane * 4);
#pragma unroll
      for (int e = 0; e < 4; ++e) { ka0[4 * g + e] += v0[e]; ka1[4 * g + e] += v1[e]; }
    }
    float kmax = fabsf(k2048);
#pragma unroll
    for (int i = 0; i < 16; ++i) kmax = fmaxf(kmax, fmaxf(fabsf(ka0[i]), fabsf(ka1[i])));
    kmax = wave_max(kmax);
    const float ksc = kmax > 0.f ? pow2_scale(kmax) : 1.f;
    _Float16* lg = (_Float16*)(cbp + AX_PAR);
#pragma unroll
    for (int i = 0; i < 16; ++i) {
      int r = (i & 3) + 8 * (i >> 2) + 4 * h;
      lg[32 * r + l31] = (_Float16)(ka0[i] * ksc);
      lg[1024 + 32 * r + l31] = (_Float16)(ka1[i] * ksc);
    }
    __builtin_amdgcn_wave_barrier();
    char* gdst = ws + WS_KG + (size_t)d * 4160;
#pragma unroll
    for (int i = 0; i < 4; ++i) *(u32x4*)(gdst + (lane + 64 * i) * 16) = *(const u32x4*)((const char*)lg + (lane + 64 * i) * 16);
    if (lane == 0) { *(_Float16*)(gdst + 4096) = (_Float16)(k2048 * ksc); ((float*)(ws + WS_KS))[d] = 1.0f / (wsc * ksc); }
  }
  STAMP(3);
}

__global__ __launch_bounds__(512, 4) void kernelA(const u16* __restrict__ U, const u16* __restrict__ Are,
    const u16* __restrict__ Aim, const u16* __restrict__ Bre, const u16* __restrict__ Bim, const u16* __restrict__ Cre,
    const u16* __restrict__ Cim, const u16* __restrict__ logdt, const u16* __restrict__ Wqkv, char* __restrict__ ws) {
  extern __shared__ __attribute__((aligned(16))) char smem[];
  const int b = blockIdx.x;
  if (b < 256)
    gemm1_block(U, Wqkv, (u16*)(ws + WS_PQ), (float*)(ws + WS_KVP), (float*)(ws + WS_ZP), smem, b);
  else
    aux_block(U, Are, Aim, Bre, Bim, Cre, Cim, logdt, ws, smem, b - 256);
}

constexpr int CV_CSTR = 4160;
constexpr int CV_COPY = 0;
constexpr int CV_UIMG = 4 * CV_CSTR;
constexpr int CV_GRID = CV_UIMG;
constexpr int CV_CH = CV_UIMG + 6080 + 32;
constexpr int CV_LDS = 2 * CV_CH + 64;

DI void conv_block(char* __restrict__ ws, char* smem, int b) {
  char* stamp_base = ws + WS_STAMP + (size_t)512 * 8 * 64 * 8; (void)stamp_base;
  STAMP(0);
  const int tid = threadIdx.x, lane = tid & 63;
  const int wave = __builtin_amdgcn_readfirstlane(tid >> 6);
  const int ch = wave >> 1, hw = wave & 1;
  const int d = b * 2 + ch;
  const int l31 = lane & 31, h = lane >> 5;
  const int pl = lane + 64 * hw;
  char* cbp = smem + ch * CV_CH;
  float* shs = (float*)(smem + 2 * CV_CH);
  const u32x4* gsrc = (const u32x4*)(ws + WS_KG + (size_t)d * 4160);
  const u32x4* usrc = (const u32x4*)(ws + WS_UT + (size_t)d * 4096);
  u32x4 gv0 = gsrc[pl], gv1 = gsrc[pl + 128], gv2 = gsrc[256 + (pl & 3)];
  u32x4 uv0 = usrc[pl], uv1 = usrc[pl + 128];
  const float kinv = ((const float*)(ws + WS_KS))[d];
  *(u32x4*)(cbp + CV_GRID + pl * 16) = gv0;
  *(u32x4*)(cbp + CV_GRID + (pl + 128) * 16) = gv1;
  if (pl < 4) *(u32x4*)(cbp + CV_GRID + (256 + pl) * 16) = gv2;
  {
    unsigned mx = 0;
#pragma unroll
    for (int j = 0; j < 4; ++j) {
      mx = max(mx, max(max(uv0[j] & 0x7fffu, (uv0[j] >> 16) & 0x7fffu), max(uv1[j] & 0x7fffu, (uv1[j] >> 16) & 0x7fffu)));
    }
    float m = wave_max(bf2f((u16)mx));
    if (lane == 0) shs[wave] = m;
  }
  __syncthreads();
  STAMP(1);
  const u16* gridp = (const u16*)(cbp + CV_GRID);
  for (int i0 = 8 * pl; i0 < 2080; i0 += 1024) {
    unsigned v[11];
#pragma unroll
    for (int e = 0; e < 11; ++e) {
      int lag = 2047 - i0 - e;
      int lb = (int)bfr((float)(lag < 0 ? 0 : lag));
      unsigned x = gridp[lb];
      v[e] = lag < 0 ? 0u : x;
    }
#pragma unroll
    for (int rho = 0; rho < 4; ++rho) {
      u32x4 o;
#pragma unroll
      for (int j = 0; j < 4; ++j) o[j] = v[rho + 2 * j] | (v[rho + 2 * j + 1] << 16);
      *(u32x4*)(cbp + CV_COPY + rho * CV_CSTR + i0 * 2) = o;
    }
  }
  __syncthreads();
  const float um = fmaxf(shs[2 * ch], shs[2 * ch + 1]);
  const float usc = um > 0.f ? pow2_scale(um) : 1.f;
  if (pl < 124) *(u32x4*)(cbp + CV_UIMG + pl * 16) = (u32x4){0, 0, 0, 0};
#pragma unroll
  for (int i = 0; i < 2; ++i) {
    u32x4 v = i ? uv1 : uv0, o;
#pragma unroll
    for (int j = 0; j < 4; ++j) o[j] = pack2h(lo_f(v[j]) * usc, hi_f(v[j]) * usc);
    int c = pl + 128 * i, row = (c >> 2) + 31;
    *(u32x4*)(cbp + CV_UIMG + row * 64 + (((c & 3) ^ ((row >> 2) & 3)) << 4)) = o;
  }
  __syncthreads();
  STAMP(2);
  f32x16 y0, y1;
#pragma unroll
  for (int i = 0; i < 16; ++i) y0[i] = y1[i] = 0.f;
  const unsigned cbase = lds_addr(cbp);
  const int rho = (2047 + 8 * h - l31) & 3;
  unsigned aaddr = cbase + CV_COPY + rho * CV_CSTR + (2047 + 16 * hw + 8 * h - l31 - rho) * 2;
  const int chk = 2 * hw + h;
  int row = l31 + 31;
  s16x4 al[3], ah[3];
  half8 b0[3], b1[3];
#define CV_RD2(slot) { \
      unsigned r0_ = cbase + CV_UIMG + row * 64 + ((chk ^ ((row >> 2) & 3)) << 4); \
      DS_B64(al[slot], aaddr, 0); DS_B64(ah[slot], aaddr, 8); DS_B128(b0[slot], r0_, 0); DS_B128(b1[slot], r0_, 2048); \
      aaddr -= 64; row -= 1; }
#define CV_RD1(slot) { \
      unsigned r0_ = cbase + CV_UIMG + row * 64 + ((chk ^ ((row >> 2) & 3)) << 4); \
      DS_B64(al[slot], aaddr, 0); DS_B64(ah[slot], aaddr, 8); DS_B128(b1[slot], r0_, 2048); \
      aaddr -= 64; row -= 1; }
#define CV_MM2(slot) { half8 af_ = cat_h(al[slot], ah[slot]); y0 = MFMA_H(af_, b0[slot], y0); y1 = MFMA_H(af_, b1[slot], y1); }
#define CV_MM1(slot) { y1 = MFMA_H(cat_h(al[slot], ah[slot]), b1[slot], y1); }
  CV_RD2(0) CV_RD2(1)
#pragma unroll 1
  for (int mm = 0; mm < 30; mm += 3) {
    CV_RD2(2) LGKM(8); CV_MM2(0) SCHED();
    CV_RD2(0) LGKM(8); CV_MM2(1) SCHED();
    CV_RD2(1) LGKM(8); CV_MM2(2) SCHED();
  }
  CV_RD1(2) LGKM(7); CV_MM2(0) SCHED();
  CV_RD1(0) LGKM(6); CV_MM2(1) SCHED();
#pragma unroll 1
  for (int mm = 32; mm < 62; mm += 3) {
    CV_RD1(1) LGKM(6); CV_MM1(2) SCHED();
    CV_RD1(2) LGKM(6); CV_MM1(0) SCHED();
    CV_RD1(0) LGKM(6); CV_MM1(1) SCHED();
  }
  LGKM(3); CV_MM1(2) SCHED();
  LGKM(0); CV_MM1(0) SCHED();
  STAMP(3);
  float* xch = (float*)(cbp + CV_COPY);
  __syncthreads();
  {
    f32x16& snd = hw == 0 ? y1 : y0;
#pragma unroll
    for (int g = 0; g < 4; ++g)
      *(f32x4*)(xch + hw * 1024 + g * 256 + lane * 4) = (f32x4){snd[4 * g], snd[4 * g + 1], snd[4 * g + 2], snd[4 * g + 3]};
  }
  __syncthreads();
  f32x16 ya = hw == 0 ? y0 : y1;
#pragma unroll
  for (int g = 0; g < 4; ++g) {
    f32x4 v = *(const f32x4*)(xch + (hw ^ 1) * 1024 + g * 256 + lane * 4);
#pragma unroll
    for (int e = 0; e < 4; ++e) ya[4 * g + e] += v[e];
  }
  const float osc = kinv / usc;
  u16* yd = (u16*)(ws + WS_YT) + (size_t)d * L_ + 1024 * hw;
#pragma unroll
  for (int g = 0; g < 4; ++g) {
    u32x2 v;
    v[0] = pack2bf(ya[4 * g] * osc, ya[4 * g + 1] * osc);
    v[1] = pack2bf(ya[4 * g + 2] * osc, ya[4 * g + 3] * osc);
    *(u32x2*)(yd + 32 * l31 + 8 * g + 4 * h) = v;
  }
  STAMP(4);
}

__global__ __launch_bounds__(256, 3) void kernelM(const u16* __restrict__ U, const u16* __restrict__ gw, char* __restrict__ ws) {
  extern __shared__ __attribute__((aligned(16))) char smem[];
  const int tid = threadIdx.x, lane = tid & 63, wave = tid >> 6;
  int b = blockIdx.x;
  if (b < 512) { conv_block(ws, smem, b); return; }
  b -= 512;
  if (b >= 256) {
    b -= 256;
    float* G = (float*)(ws + WS_G);
    u32x4 w0 = *(const u32x4*)(gw + lane * 16), w1 = *(const u32x4*)(gw + lane * 16 + 8);
    u32x4 xa[4], xb[4];
#pragma unroll
    for (int i = 0; i < 4; ++i) {
      int row = b * 16 + wave * 4 + i;
      xa[i] = *(const u32x4*)(U + (size_t)row * D_ + lane * 16);
      xb[i] = *(const u32x4*)(U + (size_t)row * D_ + lane * 16 + 8);
    }
#pragma unroll
    for (int i = 0; i < 4; ++i) {
      int row = b * 16 + wave * 4 + i;
      u32x4 x0 = xa[i], x1 = xb[i];
      float s = 0.f;
#pragma unroll
      for (int j = 0; j < 4; ++j) {
        unsigned p0 = pack2bf(lo_f(x0[j]) * lo_f(w0[j]), hi_f(x0[j]) * hi_f(w0[j]));
        unsigned p1 = pack2bf(lo_f(x1[j]) * lo_f(w1[j]), hi_f(x1[j]) * hi_f(w1[j]));
        s += (lo_f(p0) + hi_f(p0)) + (lo_f(p1) + hi_f(p1));
      }
      s = bfr(wave_sum(s));
      float g = bfr(1.0f / (1.0f + __expf(-s)));
      if (lane == 0) G[row] = g;
    }
    return;
  }
  u16* kvT = (u16*)smem;
  float* zs = (float*)(smem + 2560);
  float* zpart = (float*)(smem + 4096);
  const int head = b >> 3, chunk = b & 7;
  const int l31 = lane & 31, h = lane >> 5;
  const float* kvp = (const float*)(ws + WS_KVP);
  const float* zp = (const float*)(ws + WS_ZP);
  const u16* pq = (const u16*)(ws + WS_PQ);
  u16* o = (u16*)(ws + WS_O);
  u32x4 qv[2][2];
#pragma unroll
  for (int rt = 0; rt < 2; ++rt)
#pragma unroll
    for (int ks = 0; ks < 2; ++ks)
      qv[rt][ks] = *(const u32x4*)(pq + (size_t)(chunk * 256 + wave * 64 + rt * 32 + l31) * D_ + head * 32 + 16 * ks + 8 * h);
  {
    int e = tid >> 3, d4 = (tid & 7) * 4;
    f32x4 p[16];
#pragma unroll
    for (int r = 0; r < 16; ++r) p[r] = *(const f32x4*)(kvp + ((size_t)(r * 32 + head) * 32 + e) * 32 + d4);
    float zz0 = zp[((tid >> 5) * 32 + head) * 32 + (tid & 31)];
    float zz1 = zp[((8 + (tid >> 5)) * 32 + head) * 32 + (tid & 31)];
    f32x4 s = p[0];
#pragma unroll
    for (int r = 1; r < 16; ++r) s += p[r];
    u32x2 v;
    v[0] = pack2bf(s[0], s[1]); v[1] = pack2bf(s[2], s[3]);
    *(u32x2*)(kvT + e * 40 + d4) = v;
    zpart[tid] = zz0 + zz1;
    __syncthreads();
    if (tid < 32) {
      float z = 0.f;
#pragma unroll
      for (int r = 0; r < 8; ++r) z += zpart[r * 32 + tid];
      zs[tid] = bfr(z);
    }
  }
  __syncthreads();
  bf16x8 ka[2];
  float zr[2][8];
#pragma unroll
  for (int ks = 0; ks < 2; ++ks) {
    ka[ks] = *(const bf16x8*)(kvT + l31 * 40 + 16 * ks + 8 * h);
#pragma unroll
    for (int j = 0; j < 8; ++j) zr[ks][j] = zs[16 * ks + 8 * h + j];
  }
#pragma unroll
  for (int rt = 0; rt < 2; ++rt) {
    int s = chunk * 256 + wave * 64 + rt * 32 + l31;
    f32x16 acc;
#pragma unroll
    for (int i = 0; i < 16; ++i) acc[i] = 0.f;
    float den = 0.f;
#pragma unroll
    for (int ks = 0; ks < 2; ++ks) {
      u32x4 q = qv[rt][ks];
#pragma unroll
      for (int j = 0; j < 4; ++j) {
        den += lo_f(q[j]) * zr[ks][2 * j];
        den += hi_f(q[j]) * zr[ks][2 * j + 1];
      }
      acc = MFMA_BF(ka[ks], __builtin_bit_cast(bf16x8, q), acc);
    }
    den += __shfl_xor(den, 32);
    den = bfr(bfr(den) + 1e-6f);
    float rd = 1.0f / den;
#pragma unroll
    for (int g = 0; g < 4; ++g) {
      unsigned n0 = pack2bf(acc[4 * g], acc[4 * g + 1]), n1 = pack2bf(acc[4 * g + 2], acc[4 * g + 3]);
      u32x2 v;
      v[0] = pack2bf(lo_f(n0) * rd, hi_f(n0) * rd);
      v[1] = pack2bf(lo_f(n1) * rd, hi_f(n1) * rd);
      *(u32x2*)(o + (size_t)s * D_ + head * 32 + 8 * g + 4 * h) = v;
    }
  }
}

constexpr int B_STAGE = 24576, B_NST = 6;
__global__ __launch_bounds__(512) void kernelB(const u16* __restrict__ U, const u16* __restrict__ Dskip,
                                                 const u16* __restrict__ Wo, const char* __restrict__ ws, u16* __restrict__ out) {
  extern __shared__ __attribute__((aligned(16))) char smem[];
  const int b = blockIdx.x, xcd = b & 7, idx = b >> 3;
  const int rb = (xcd >> 1) * 4 + (idx & 3);
  const int cbk = (xcd & 1) * 8 + (idx >> 2);
  const int tid = threadIdx.x, lane = tid & 63;
  const int wave = __builtin_amdgcn_readfirstlane(tid >> 6);
  const int wm = wave & 3, wn = wave >> 2;
  const int l31 = lane & 31, h = lane >> 5;
  const u16* O = (const u16*)(ws + WS_O);
  char* stamp_base = (char*)ws + WS_STAMP + (size_t)1024 * 8 * 64 * 8; (void)stamp_base;
  STAMP(0);
  const u16* ga[2];
  const u16* gb;
#pragma unroll
  for (int i = 0; i < 2; ++i) {
    int row = 16 * wave + 8 * i + (lane >> 3);
    int chunk = (lane & 7) ^ ((row >> 1) & 7);
    ga[i] = O + (size_t)(rb * 128 + row) * D_ + chunk * 8;
  }
  {
    int kr = 8 * wave + (lane >> 3);
    int chunk = (lane & 7) ^ (((kr >> 1) & 1) << 2);
    gb = Wo + (size_t)kr * D_ + cbk * 64 + chunk * 8;
  }
  auto stage = [&](int kt, int buf) {
    char* base = smem + buf * B_STAGE;
    glds16(ga[0] + kt * 64, base + (16 * wave) * 128);
    glds16(ga[1] + kt * 64, base + (16 * wave + 8) * 128);
    glds16(gb + (size_t)kt * 64 * D_, base + 16384 + wave * 1024);
  };
  const unsigned sb = lds_addr(smem);
  unsigned aad[4];
  {
    int row = 32 * wm + l31;
#pragma unroll
    for (int ks = 0; ks < 4; ++ks) aad[ks] = sb + row * 128 + (((2 * ks + h) ^ ((row >> 1) & 7)) << 4);
  }
  const int blk = (lane >> 4) & 1, q4 = (lane & 15) >> 2, p4 = lane & 3;
  const unsigned bad = sb + 16384 + (8 * h + q4) * 128 + ((wn ^ (q4 >> 1)) << 6) + blk * 32 + p4 * 8;
  f32x16 acc;
#pragma unroll
  for (int i = 0; i < 16; ++i) acc[i] = 0.f;
#pragma unroll
  for (int s = 0; s < 6; ++s) stage(s, s);
  const int dcol = cbk * 64 + wn * 32 + l31;
  const int trow = rb * 128 + 32 * wm + 4 * h;
  const float dsk = bf2f(Dskip[dcol]);
  const float* G = (const float*)(ws + WS_G);
  const u16* yT = (const u16*)(ws + WS_YT) + (size_t)dcol * L_;
  u32x2 yv[4]; f32x4 gv[4]; u16 uu[16];
#pragma unroll
  for (int g = 0; g < 4; ++g) {
    yv[g] = *(const u32x2*)(yT + trow + 8 * g);
    gv[g] = *(const f32x4*)(G + trow + 8 * g);
#pragma unroll
    for (int e = 0; e < 4; ++e) uu[4 * g + e] = U[(size_t)(trow + 8 * g + e) * D_ + dcol];
  }
  unsigned aadh[4], aadh2[4];
#pragma unroll
  for (int ks = 0; ks < 4; ++ks) { aadh[ks] = aad[ks] + 2 * B_STAGE; aadh2[ks] = aad[ks] + 4 * B_STAGE; }
  const unsigned badh = bad + 2 * B_STAGE, badh2 = bad + 4 * B_STAGE;
  bf16x8 a[2][4]; s16x4 bl[2][4], bh[2][4];
#define B_RD2(S, ks, AA, BB, bo) DS_B128(a[S][ks], AA[ks], bo); DS_TR(bl[S][ks], BB, bo + ks * 2048); DS_TR(bh[S][ks], BB, bo + ks * 2048 + 512);
#define B_RD(S, ks, buf) if constexpr ((buf) < 2) { B_RD2(S, ks, aad, bad, (buf) * B_STAGE) } else if constexpr ((buf) < 4) { B_RD2(S, ks, aadh, badh, ((buf) - 2) * B_STAGE) } else { B_RD2(S, ks, aadh2, badh2, ((buf) - 4) * B_STAGE) }
#define B_MM(S, ks) { acc = MFMA_BF(a[S][ks], cat_bf(bl[S][ks], bh[S][ks]), acc); }
#define B_STEP(kt, buf, S, WAITN) { \
    asm volatile("s_waitcnt vmcnt(" #WAITN ") lgkmcnt(0)" ::: "memory"); \
    __builtin_amdgcn_s_barrier(); \
    STAMP(2 + (kt)); \
    if ((kt) + 6 < 16) stage((kt) + 6, buf); \
    if ((kt) < 15) { B_RD((S) ^ 1, 0, ((buf) + 1) % 6) B_RD((S) ^ 1, 1, ((buf) + 1) % 6) } \
    SCHED(); B_MM(S, 0) B_MM(S, 1) SCHED(); \
    if ((kt) < 15) { B_RD((S) ^ 1, 2, ((buf) + 1) % 6) B_RD((S) ^ 1, 3, ((buf) + 1) % 6) } \
    SCHED(); B_MM(S, 2) B_MM(S, 3) asm volatile("" : "+v"(acc)); SCHED(); }
  asm volatile("s_waitcnt vmcnt(40)" ::: "memory");
  __builtin_amdgcn_s_barrier();
  STAMP(1);
  B_RD(0, 0, 0) B_RD(0, 1, 0) B_RD(0, 2, 0) B_RD(0, 3, 0)
  B_STEP(0, 0, 0, 37) B_STEP(1, 1, 1, 37) B_STEP(2, 2, 0, 37) B_STEP(3, 3, 1, 37) B_STEP(4, 4, 0, 37)
  B_STEP(5, 5, 1, 12) B_STEP(6, 0, 0, 12) B_STEP(7, 1, 1, 12) B_STEP(8, 2, 0, 12) B_STEP(9, 3, 1, 12) B_STEP(10, 4, 0, 12)
  B_STEP(11, 5, 1, 9) B_STEP(12, 0, 0, 6) B_STEP(13, 1, 1, 3) B_STEP(14, 2, 0, 0) B_STEP(15, 3, 1, 0)
#pragma unroll
  for (int g = 0; g < 4; ++g) {
#pragma unroll
    for (int e2 = 0; e2 < 2; ++e2) {
      unsigned ud = pack2bf(bf2f(uu[4 * g + 2 * e2]) * dsk, bf2f(uu[4 * g + 2 * e2 + 1]) * dsk);
      unsigned a = pack2bf(lo_f(yv[g][e2]) + lo_f(ud), hi_f(yv[g][e2]) + hi_f(ud));
      unsigned bb = pack2bf(acc[4 * g + 2 * e2], acc[4 * g + 2 * e2 + 1]);
      float g0 = gv[g][2 * e2], g1 = gv[g][2 * e2 + 1];
      unsigned og = pack2bf(1.0f - g0, 1.0f - g1);
      unsigned ga_ = pack2bf(g0 * lo_f(a), g1 * hi_f(a));
      unsigned gb_ = pack2bf(lo_f(og) * lo_f(bb), hi_f(og) * hi_f(bb));
      unsigned r = pack2bf(lo_f(ga_) + lo_f(gb_), hi_f(ga_) + hi_f(gb_));
      int t = trow + 8 * g + 2 * e2;
      out[(size_t)t * D_ + dcol] = (u16)(r & 0xffffu);
      out[(size_t)(t + 1) * D_ + dcol] = (u16)(r >> 16);
    }
  }
  STAMP(20);
}

extern "C" void kernel_launch(void* const* d_in, const int* in_sizes, int n_in, void* d_out, int out_size, void* d_ws,
                              size_t ws_size, hipStream_t stream) {
  const u16* U = (const u16*)d_in[0];
  char* ws = (char*)d_ws;
  (void)hipFuncSetAttribute((const void*)kernelA, hipFuncAttributeMaxDynamicSharedMemorySize, A_LDS);
  (void)hipFuncSetAttribute((const void*)kernelM, hipFuncAttributeMaxDynamicSharedMemorySize, CV_LDS);
  (void)hipFuncSetAttribute((const void*)kernelB, hipFuncAttributeMaxDynamicSharedMemorySize, B_STAGE * B_NST);
  kernelA<<<512, 512, A_LDS, stream>>>(U, (const u16*)d_in[1], (const u16*)d_in[2], (const u16*)d_in[3],
                                       (const u16*)d_in[4], (const u16*)d_in[5], (const u16*)d_in[6],
                                       (const u16*)d_in[7], (const u16*)d_in[10], ws);
  kernelM<<<512 + 256 + 128, 256, CV_LDS, stream>>>(U, (const u16*)d_in[12], ws);
  kernelB<<<256, 512, B_STAGE * B_NST, stream>>>(U, (const u16*)d_in[8], (const u16*)d_in[11], ws, (u16*)d_out);
}
```

```cpp
#include <hip/hip_runtime.h>
#include <hip/hip_bf16.h>
#include <stdint.h>

typedef __attribute__((ext_vector_type(8))) __bf16 bf16x8;
typedef __attribute__((ext_vector_type(8))) _Float16 half8;
typedef __attribute__((ext_vector_type(4))) _Float16 half4;
typedef __attribute__((ext_vector_type(2))) _Float16 half2v;
typedef __attribute__((ext_vector_type(4))) short s16x4;
typedef __attribute__((ext_vector_type(8))) short s16x8;
typedef __attribute__((ext_vector_type(16))) float f32x16;
typedef __attribute__((ext_vector_type(4))) float f32x4;
typedef __attribute__((ext_vector_type(2))) float f32x2;
typedef __attribute__((ext_vector_type(4))) unsigned u32x4;
typedef __attribute__((ext_vector_type(2))) unsigned u32x2;
typedef unsigned short u16;

#define DI __device__ __forceinline__
#define LDS_AS __attribute__((address_space(3)))

constexpr int L_ = 2048, D_ = 1024, NST = 128;

DI float bf2f(u16 x) { return __uint_as_float(((unsigned)x) << 16); }
DI u16 f2bf(float x) { unsigned u = __float_as_uint(x); u += 0x7fffu + ((u >> 16) & 1u); return (u16)(u >> 16); }
DI float bfr(float x) { unsigned u = __float_as_uint(x); u += 0x7fffu + ((u >> 16) & 1u); return __uint_as_float(u & 0xffff0000u); }
typedef __attribute__((ext_vector_type(2))) __bf16 bf16x2v;
DI unsigned pack2bf(float lo, float hi) { f32x2 v = {lo, hi}; return __builtin_bit_cast(unsigned, __builtin_convertvector(v, bf16x2v)); }
DI float lo_f(unsigned p) { return __uint_as_float(p << 16); }
DI float hi_f(unsigned p) { return __uint_as_float(p & 0xffff0000u); }
DI unsigned pack2h(float lo, float hi) { f32x2 v = {lo, hi}; return __builtin_bit_cast(unsigned, __builtin_convertvector(v, half2v)); }

DI void glds16(const void* g, char* l) {
  __builtin_amdgcn_global_load_lds((const __attribute__((address_space(1))) void*)g, (LDS_AS void*)l, 16, 0, 0);
}
DI bf16x8 cat_bf(s16x4 a, s16x4 b) {
  s16x8 r = __builtin_shufflevector(a, b, 0, 1, 2, 3, 4, 5, 6, 7);
  return __builtin_bit_cast(bf16x8, r);
}
DI half8 cat_h(s16x4 a, s16x4 b) {
  s16x8 r = __builtin_shufflevector(a, b, 0, 1, 2, 3, 4, 5, 6, 7);
  return __builtin_bit_cast(half8, r);
}
DI unsigned lds_addr(const char* p) { return (unsigned)(size_t)(LDS_AS const char*)p; }
#define DS_B128(dst, addr, off) asm volatile("ds_read_b128 %0, %1 offset:%2" : "=v"(dst) : "v"(addr), "i"(off) : "memory")
#define DS_B64(dst, addr, off) asm volatile("ds_read_b64 %0, %1 offset:%2" : "=v"(dst) : "v"(addr), "i"(off) : "memory")
#define DS_TR(dst, addr, off) asm volatile("ds_read_b64_tr_b16 %0, %1 offset:%2" : "=v"(dst) : "v"(addr), "i"(off) : "memory")
#define LGKM(n) do { asm volatile("s_waitcnt lgkmcnt(" #n ")" ::: "memory"); __builtin_amdgcn_sched_barrier(0); } while (0)
#define SCHED() __builtin_amdgcn_sched_barrier(0)
#define MFMA_BF(a, b, c) __builtin_amdgcn_mfma_f32_32x32x16_bf16((a), (b), (c), 0, 0, 0)
#define MFMA_H(a, b, c) __builtin_amdgcn_mfma_f32_32x32x16_f16((a), (b), (c), 0, 0, 0)

DI unsigned phi2(float a0, float a1) {
  unsigned t = pack2bf(a0, a1);
  float t0 = lo_f(t), t1 = hi_f(t);
  float r0 = t0 > 0.f ? t0 + 1.0f : __builtin_amdgcn_exp2f(t0 * 1.44269504088896f);
  float r1 = t1 > 0.f ? t1 + 1.0f : __builtin_amdgcn_exp2f(t1 * 1.44269504088896f);
  unsigned r = pack2bf(r0, r1);
  return pack2bf(lo_f(r) + 1e-6f, hi_f(r) + 1e-6f);
}

constexpr size_t WS_PQ = 0;
constexpr size_t WS_O = WS_PQ + (size_t)L_ * D_ * 2;
constexpr size_t WS_YT = WS_O + (size_t)L_ * D_ * 2;
constexpr size_t WS_KVP = WS_YT + (size_t)L_ * D_ * 2;
constexpr size_t WS_ZP = WS_KVP + (size_t)16 * 32 * 1024 * 4;
constexpr size_t WS_G = WS_ZP + (size_t)16 * 32 * 32 * 4;
constexpr size_t WS_UT = WS_G + 8192;
constexpr size_t WS_KG = WS_UT + (size_t)L_ * D_ * 2;
constexpr size_t WS_KS = WS_KG + (size_t)D_ * 1296;
constexpr int A_LDS = 80 * 1024;

constexpr int G1_STAGE = 20480;
DI void gemm1_block(const u16* __restrict__ X, const u16* __restrict__ W, u16* __restrict__ pq, float* __restrict__ kvp,
                    float* __restrict__ zp, char* smem, int b) {

  const int xcd = b & 7, idx = b >> 3;
  const int rb = (xcd >> 2) * 8 + (idx & 7);
  const int hp = (xcd & 3) * 4 + (idx >> 3);
  const int tid = threadIdx.x, lane = tid & 63;
  const int wave = __builtin_amdgcn_readfirstlane(tid >> 6);
  const int wm = wave & 3, hh = wave >> 2;
  const int l31 = lane & 31, h = lane >> 5;
  const u16* ga;
  {
    int row = 16 * wave + (lane >> 2);
    int chunk = (lane & 3) ^ ((row >> 2) & 3);
    ga = X + (size_t)(rb * 128 + row) * D_ + chunk * 8;
  }
  const u16* gbl;
  {
    int kr = lane >> 3;
    int chunk = (lane & 7) ^ (((kr >> 1) & 1) << 2);
    gbl = W + (size_t)kr * 3072 + hp * 64 + chunk * 8;
  }
  auto stage = [&](int kt) {
    char* base = smem + (kt & 3) * G1_STAGE;
    glds16(ga + kt * 32, base + wave * 1024);
    int j0 = (wave + 4 * kt) & 7;
    {
      int slab = j0 >> 2, part = j0 & 3;
      glds16(gbl + (size_t)(kt * 32 + part * 8) * 3072 + slab * 1024, base + 8192 + j0 * 1024);
    }
    if (j0 < 4) glds16(gbl + (size_t)(kt * 32 + j0 * 8) * 3072 + 2048, base + 8192 + (8 + j0) * 1024);
  };
  const unsigned sb = lds_addr(smem);
  unsigned aad[2];
  {
    int row = 32 * wm + l31;
#pragma unroll
    for (int ks = 0; ks < 2; ++ks) aad[ks] = sb + row * 64 + (((2 * ks + h) ^ ((row >> 2) & 3)) << 4);
  }
  const int blk = (lane >> 4) & 1, q4 = (lane & 15) >> 2, p4 = lane & 3;
  const unsigned bad = sb + 8192 + (8 * h + q4) * 128 + ((hh ^ (q4 >> 1)) << 6) + blk * 32 + p4 * 8;

  f32x16 accq, acck, accv;
#pragma unroll
  for (int i = 0; i < 16; ++i) accq[i] = acck[i] = accv[i] = 0.f;

  const unsigned aadh[2] = {aad[0] + 2 * G1_STAGE, aad[1] + 2 * G1_STAGE};
  const unsigned badh = bad + 2 * G1_STAGE;
  bf16x8 a[2]; s16x4 bl[2][3], bh[2][3];
#define G1_RD2(ks, AA, BB, bo) \
  DS_B128(a[ks], AA[ks], bo); \
  DS_TR(bl[ks][0], BB, bo + ks * 2048); DS_TR(bh[ks][0], BB, bo + ks * 2048 + 512); \
  DS_TR(bl[ks][1], BB, bo + ks * 2048 + 4096); DS_TR(bh[ks][1], BB, bo + ks * 2048 + 4096 + 512); \
  DS_TR(bl[ks][2], BB, bo + ks * 2048 + 8192); DS_TR(bh[ks][2], BB, bo + ks * 2048 + 8192 + 512);
#define G1_RD(ks, buf) if constexpr ((buf) < 2) { G1_RD2(ks, aad, bad, (buf) * G1_STAGE) } else { G1_RD2(ks, aadh, badh, ((buf) - 2) * G1_STAGE) }
#define G1_MM(ks) { \
  accq = MFMA_BF(cat_bf(bl[ks][0], bh[ks][0]), a[ks], accq); \
  acck = MFMA_BF(a[ks], cat_bf(bl[ks][1], bh[ks][1]), acck); \
  accv = MFMA_BF(a[ks], cat_bf(bl[ks][2], bh[ks][2]), accv); }
#define G1_STEP(kt, buf, WAITN, MORE) { \
    G1_RD(1, buf) \
    LGKM(7); G1_MM(0) SCHED(); \
    asm volatile("s_waitcnt vmcnt(" #WAITN ") lgkmcnt(0)" ::: "memory"); \
    __builtin_amdgcn_s_barrier(); \
    if ((kt) + 4 < 32) stage((kt) + 4); \
    if (MORE) { G1_RD(0, ((buf) + 1) & 3) } \
    SCHED(); G1_MM(1) asm volatile("" : "+v"(accq), "+v"(acck), "+v"(accv)); SCHED(); }
  stage(0); stage(1); stage(2); stage(3);
  asm volatile("s_waitcnt vmcnt(7)" ::: "memory");
  __builtin_amdgcn_s_barrier();
  G1_RD(0, 0)
#pragma unroll 1
  for (int kt = 0; kt < 28; kt += 4) {
    G1_STEP(kt, 0, 5, 1) G1_STEP(kt + 1, 1, 5, 1) G1_STEP(kt + 2, 2, 5, 1) G1_STEP(kt + 3, 3, 5, 1)
  }
  G1_STEP(28, 0, 5, 1) G1_STEP(29, 1, 0, 1) G1_STEP(30, 2, 0, 1) G1_STEP(31, 3, 0, 0)

  const int head = hp * 2 + hh;
  {
    int s = rb * 128 + 32 * wm + l31;
#pragma unroll
    for (int g = 0; g < 4; ++g) {
      u32x2 v;
      v[0] = phi2(accq[4 * g], accq[4 * g + 1]);
      v[1] = phi2(accq[4 * g + 2], accq[4 * g + 3]);
      *(u32x2*)(pq + (size_t)s * D_ + head * 32 + 8 * g + 4 * h) = v;
    }
  }
  f32x16 kvacc;
#pragma unroll
  for (int i = 0; i < 16; ++i) kvacc[i] = 0.f;
  float zsum = 0.f;
#pragma unroll
  for (int s = 0; s < 2; ++s) {
    u32x4 pa, pb;
#pragma unroll
    for (int j = 0; j < 4; ++j) {
      pa[j] = phi2(acck[8 * s + 2 * j], acck[8 * s + 2 * j + 1]);
      zsum += lo_f(pa[j]) + hi_f(pa[j]);
      pb[j] = pack2bf(accv[8 * s + 2 * j], accv[8 * s + 2 * j + 1]);
    }
    kvacc = MFMA_BF(__builtin_bit_cast(bf16x8, pa), __builtin_bit_cast(bf16x8, pb), kvacc);
  }
  zsum += __shfl_xor(zsum, 32);
  __syncthreads();
  float* red = (float*)smem + (hh * 3 + wm - 1) * 1088;
  if (wm != 0) {
#pragma unroll
    for (int g = 0; g < 4; ++g) {
      f32x4 v = {kvacc[4 * g], kvacc[4 * g + 1], kvacc[4 * g + 2], kvacc[4 * g + 3]};
      *(f32x4*)(red + g * 256 + lane * 4) = v;
    }
    if (lane < 32) red[1024 + lane] = zsum;
  }
  __syncthreads();
  if (wm == 0) {
    float* dst = kvp + ((size_t)(rb * 32 + head) * 32 + l31) * 32 + 4 * h;
    const float* r0 = (const float*)smem + hh * 3 * 1088;
#pragma unroll
    for (int g = 0; g < 4; ++g) {
      f32x4 v = {kvacc[4 * g], kvacc[4 * g + 1], kvacc[4 * g + 2], kvacc[4 * g + 3]};
#pragma unroll
      for (int w = 0; w < 3; ++w) v += *(const f32x4*)(r0 + w * 1088 + g * 256 + lane * 4);
      *(f32x4*)(dst + 8 * g) = v;
    }
    if (lane < 32) zp[(rb * 32 + head) * 32 + lane] = zsum + r0[1024 + lane] + r0[1088 + 1024 + lane] + r0[2 * 1088 + 1024 + lane];
  }

}

DI float wave_max(float v) {
#pragma unroll
  for (int o = 32; o > 0; o >>= 1) v = fmaxf(v, __shfl_xor(v, o));
  return v;
}
DI float wave_sum(float v) {
#pragma unroll
  for (int o = 32; o > 0; o >>= 1) v += __shfl_xor(v, o);
  return v;
}
DI float pow2_scale(float m) {
  int e = (int)((__float_as_uint(m) >> 23) & 0xff);
  int se = 127 + 13 - (e - 127);
  se = se < 1 ? 1 : (se > 254 ? 254 : se);
  return __uint_as_float((unsigned)se << 23);
}
DI float ex2(float x) { return __builtin_amdgcn_exp2f(x); }
DI float sin_rev(float x) { return __builtin_amdgcn_sinf(__builtin_amdgcn_fractf(x)); }
DI float cos_rev(float x) { return __builtin_amdgcn_cosf(__builtin_amdgcn_fractf(x)); }

constexpr int AX_PAR = 0;
constexpr int AX_XCH = 4160;
constexpr int AX_CH = 12352;
constexpr int AX_TR = 4 * AX_CH;

DI void aux_block(const u16* __restrict__ U, const u16* __restrict__ Are, const u16* __restrict__ Aim,
                  const u16* __restrict__ Bre, const u16* __restrict__ Bim, const u16* __restrict__ Cre,
                  const u16* __restrict__ Cim, const u16* __restrict__ logdt, char* __restrict__ ws, char* smem, int b) {

  const int tid = threadIdx.x, lane = tid & 63;
  const int wave = __builtin_amdgcn_readfirstlane(tid >> 6);
  const int ch = wave >> 1, hw = wave & 1;
  const int d = b * 4 + ch;
  const int l31 = lane & 31, h = lane >> 5;
  char* cbp = smem + ch * AX_CH;
  const int tr0 = (b >> 4) * 128, tc0 = (b & 15) * 64;
  u32x4 ut[2];
#pragma unroll
  for (int i = 0; i < 2; ++i) {
    int e = tid + 512 * i;
    ut[i] = *(const u32x4*)(U + (size_t)(tr0 + (e >> 3)) * D_ + tc0 + (e & 7) * 8);
  }
  const float dt = bfr(__expf(bf2f(logdt[d])));
  float pa[2], pb[2], pwr[2], pwi[2], wmax = 0.f;
#pragma unroll
  for (int i = 0; i < 2; ++i) {
    int n = lane + 64 * i;
    float are = bf2f(Are[d * NST + n]), aim = bf2f(Aim[d * NST + n]);
    float bre = dt * bf2f(Bre[d * NST + n]), bim = dt * bf2f(Bim[d * NST + n]);
    float cre = bf2f(Cre[d * NST + n]), cim = bf2f(Cim[d * NST + n]);
    pa[i] = dt * are * 1.44269504088896f;
    pb[i] = dt * aim * 0.159154943091895f;
    pwr[i] = cre * bre - cim * bim;
    pwi[i] = cre * bim + cim * bre;
    wmax = fmaxf(wmax, fmaxf(fabsf(pwr[i]), fabsf(pwi[i])));
  }
  wmax = wave_max(wmax);
  const float wsc = wmax > 0.f ? pow2_scale(wmax) : 1.f;
  float k2048 = 0.f;
  float* par = (float*)(cbp + AX_PAR);
#pragma unroll
  for (int i = 0; i < 2; ++i) {
    float wr = pwr[i] * wsc, wi = pwi[i] * wsc;
    float e1 = ex2(pa[i] * 1024.f), c1 = cos_rev(pb[i] * 1024.f), s1 = sin_rev(pb[i] * 1024.f);
    float er = e1 * c1, ei = e1 * s1;
    float e2r = er * er - ei * ei, e2i = 2.f * er * ei;
    k2048 += wr * e2r - wi * e2i;
    if (i == hw) {
      int n = lane + 64 * i;
      *(f32x4*)(par + n * 8) = (f32x4){pa[i], pb[i], wr, wi};
      *(f32x4*)(par + n * 8 + 4) = (f32x4){er, ei, 0.f, 0.f};
    }
  }
  k2048 = wave_sum(k2048);

  f32x16 ka0, ka1;
#pragma unroll
  for (int i = 0; i < 16; ++i) ka0[i] = ka1[i] = 0.f;
  const float rf = (float)(32 * l31), cf = (float)l31;
  unsigned paddr = lds_addr(cbp + AX_PAR) + (64 * hw + 4 * h) * 32;
  f32x4 P0[2][4]; f32x2 P1[2][4];
#define KG_RD(S) { _Pragma("unroll") for (int jj = 0; jj < 4; ++jj) { DS_B128(P0[S][jj], paddr, jj * 32); DS_B64(P1[S][jj], paddr, jj * 32 + 16); } paddr += 256; }
#define KG_DO(S) { u32x4 fa0, fa1, fb; \
    _Pragma("unroll") for (int jj = 0; jj < 4; ++jj) { \
      f32x4 v0 = P0[S][jj]; f32x2 v1 = P1[S][jj]; \
      float ex = ex2(v0[0] * rf), ph = v0[1] * rf; \
      float cx = ex * cos_rev(ph), sx = ex * sin_rev(ph); \
      float xr = v0[2] * cx - v0[3] * sx, xi = v0[2] * sx + v0[3] * cx; \
      float x1r = xr * v1[0] - xi * v1[1], x1i = xr * v1[1] + xi * v1[0]; \
      float ey = ex2(v0[0] * cf), py = v0[1] * cf; \
      fa0[jj] = pack2h(xr, -xi); fa1[jj] = pack2h(x1r, -x1i); fb[jj] = pack2h(ey * cos_rev(py), ey * sin_rev(py)); } \
    ka0 = MFMA_H(__builtin_bit_cast(half8, fa0), __builtin_bit_cast(half8, fb), ka0); \
    ka1 = MFMA_H(__builtin_bit_cast(half8, fa1), __builtin_bit_cast(half8, fb), ka1); }
  asm volatile("s_waitcnt lgkmcnt(0)" ::: "memory");
  KG_RD(0)
#pragma unroll 1
  for (int t = 0; t < 8; t += 2) {
    KG_RD(1) LGKM(8); KG_DO(0) SCHED();
    if (t + 2 < 8) { KG_RD(0) LGKM(8); } else { LGKM(0); }
    KG_DO(1) SCHED();
  }

  float* xch = (float*)(cbp + AX_XCH);
  if (hw == 1) {
#pragma unroll
    for (int g = 0; g < 4; ++g) {
      *(f32x4*)(xch + g * 256 + lane * 4) = (f32x4){ka0[4 * g], ka0[4 * g + 1], ka0[4 * g + 2], ka0[4 * g + 3]};
      *(f32x4*)(xch + 1024 + g * 256 + lane * 4) = (f32x4){ka1[4 * g], ka1[4 * g + 1], ka1[4 * g + 2], ka1[4 * g + 3]};
    }
  }
#pragma unroll
  for (int i = 0; i < 2; ++i) {
    int e = tid + 512 * i, row = e >> 3, c8 = (e & 7) * 8;
#pragma unroll
    for (int j = 0; j < 4; ++j) {
      *(u16*)(smem + AX_TR + (c8 + 2 * j) * 272 + row * 2) = (u16)(ut[i][j] & 0xffffu);
      *(u16*)(smem + AX_TR + (c8 + 2 * j + 1) * 272 + row * 2) = (u16)(ut[i][j] >> 16);
    }
  }
  __syncthreads();
  {
    u16* uT = (u16*)(ws + WS_UT);
#pragma unroll
    for (int i = 0; i < 2; ++i) {
      int e = tid + 512 * i, c = e >> 4, t8 = (e & 15) * 8;
      u32x4 v = *(const u32x4*)(smem + AX_TR + c * 272 + t8 * 2);
      __builtin_nontemporal_store(v, (u32x4*)(uT + (size_t)(tc0 + c) * L_ + tr0 + t8));
    }
  }
  if (hw == 0) {
#pragma unroll
    for (int g = 0; g < 4; ++g) {
      f32x4 v0 = *(const f32x4*)(xch + g * 256 + lane * 4), v1 = *(const f32x4*)(xch + 1024 + g * 256 + lane * 4);
#pragma unroll
      for (int e = 0; e < 4; ++e) { ka0[4 * g + e] += v0[e]; ka1[4 * g + e] += v1[e]; }
    }
    float kmax = fabsf(k2048);
#pragma unroll
    for (int i = 0; i < 16; ++i) kmax = fmaxf(kmax, fmaxf(fabsf(ka0[i]), fabsf(ka1[i])));
    kmax = wave_max(kmax);
    const float ksc = kmax > 0.f ? pow2_scale(kmax) : 1.f;
    _Float16* lg = (_Float16*)(cbp + AX_PAR);
#pragma unroll
    for (int i = 0; i < 16; ++i) {
      int r = (i & 3) + 8 * (i >> 2) + 4 * h;
      lg[32 * r + l31] = (_Float16)(ka0[i] * ksc);
      lg[1024 + 32 * r + l31] = (_Float16)(ka1[i] * ksc);
    }
    if (lane == 0) lg[2048] = (_Float16)(k2048 * ksc);
    __builtin_amdgcn_wave_barrier();
    char* gdst = ws + WS_KG + (size_t)d * 1296;
    const u16* lgu = (const u16*)lg;
#pragma unroll
    for (int i = 0; i < 2; ++i) {
      int c0 = 8 * (lane + 64 * i);
      if (c0 < 648) {
        u32x4 o;
#pragma unroll
        for (int j = 0; j < 4; ++j) {
          unsigned w2[2];
#pragma unroll
          for (int e = 0; e < 2; ++e) {
            int c = c0 + 2 * j + e; c = c > 640 ? 640 : c;
            int sh = c < 256 ? 0 : (c >> 7) - 1;
            w2[e] = lgu[(c - 128 * sh) << sh];
          }
          o[j] = w2[0] | (w2[1] << 16);
        }
        *(u32x4*)(gdst + c0 * 2) = o;
      }
    }
    if (lane == 0) ((float*)(ws + WS_KS))[d] = 1.0f / (wsc * ksc);
  }

}

__global__ __launch_bounds__(512, 4) void kernelA(const u16* __restrict__ U, const u16* __restrict__ Are,
    const u16* __restrict__ Aim, const u16* __restrict__ Bre, const u16* __restrict__ Bim, const u16* __restrict__ Cre,
    const u16* __restrict__ Cim, const u16* __restrict__ logdt, const u16* __restrict__ Wqkv, char* __restrict__ ws) {
  extern __shared__ __attribute__((aligned(16))) char smem[];
  const int b = blockIdx.x;
  if (b < 256)
    gemm1_block(U, Wqkv, (u16*)(ws + WS_PQ), (float*)(ws + WS_KVP), (float*)(ws + WS_ZP), smem, b);
  else
    aux_block(U, Are, Aim, Bre, Bim, Cre, Cim, logdt, ws, smem, b - 256);
}

constexpr int CV_CSTR = 4160;
constexpr int CV_COPY = 0;
constexpr int CV_UIMG = 4 * CV_CSTR;
constexpr int CV_GRID = CV_UIMG;
constexpr int CV_CH = CV_UIMG + 6080 + 32;
constexpr int CV_LDS = 2 * CV_CH + 64;

DI void conv_block(char* __restrict__ ws, char* smem, int b) {

  const int tid = threadIdx.x, lane = tid & 63;
  const int wave = __builtin_amdgcn_readfirstlane(tid >> 6);
  const int ch = wave >> 1, hw = wave & 1;
  const int d = b * 2 + ch;
  const int l31 = lane & 31, h = lane >> 5;
  const int pl = lane + 64 * hw;
  char* cbp = smem + ch * CV_CH;
  float* shs = (float*)(smem + 2 * CV_CH);
  const u32x4* gsrc = (const u32x4*)(ws + WS_KG + (size_t)d * 1296);
  const u32x4* usrc = (const u32x4*)(ws + WS_UT + (size_t)d * 4096);
  u32x4 gv0 = gsrc[pl < 81 ? pl : 80];
  u32x4 uv0 = usrc[pl], uv1 = usrc[pl + 128];
  const float kinv = ((const float*)(ws + WS_KS))[d];
  if (pl < 81) *(u32x4*)(cbp + CV_GRID + pl * 16) = gv0;
  {
    unsigned mx = 0;
#pragma unroll
    for (int j = 0; j < 4; ++j) {
      mx = max(mx, max(max(uv0[j] & 0x7fffu, (uv0[j] >> 16) & 0x7fffu), max(uv1[j] & 0x7fffu, (uv1[j] >> 16) & 0x7fffu)));
    }
    float m = wave_max(bf2f((u16)mx));
    if (lane == 0) shs[wave] = m;
  }
  __syncthreads();

  const u16* gridp = (const u16*)(cbp + CV_GRID);
  {
    unsigned v[2][11];
#pragma unroll
    for (int it = 0; it < 2; ++it)
#pragma unroll
      for (int e = 0; e < 11; ++e) {
        int lag = 2047 - (8 * pl + 1024 * it) - e;
        int lc = lag < 0 ? 0 : lag;
        int sh = 24 - __builtin_clz(lc | 128);
        int half_ = (1 << sh) >> 1;
        int ci = sh == 0 ? lc : 128 * sh + ((lc + half_ - 1 + ((lc >> sh) & 1)) >> sh);
        unsigned x = gridp[ci];
        v[it][e] = lag < 0 ? 0u : x;
      }
#pragma unroll
    for (int it = 0; it < 2; ++it)
#pragma unroll
      for (int rho = 0; rho < 4; ++rho) {
        u32x4 o;
#pragma unroll
        for (int j = 0; j < 4; ++j) o[j] = v[it][rho + 2 * j] | (v[it][rho + 2 * j + 1] << 16);
        *(u32x4*)(cbp + CV_COPY + rho * CV_CSTR + (8 * pl + 1024 * it) * 2) = o;
      }
    if (pl < 16) *(u32x4*)(cbp + CV_COPY + (pl >> 2) * CV_CSTR + (2048 + 8 * (pl & 3)) * 2) = (u32x4){0, 0, 0, 0};
  }
  __syncthreads();
  const float um = fmaxf(shs[2 * ch], shs[2 * ch + 1]);
  const float usc = um > 0.f ? pow2_scale(um) : 1.f;
  if (pl < 124) *(u32x4*)(cbp + CV_UIMG + pl * 16) = (u32x4){0, 0, 0, 0};
#pragma unroll
  for (int i = 0; i < 2; ++i) {
    u32x4 v = i ? uv1 : uv0, o;
#pragma unroll
    for (int j = 0; j < 4; ++j) o[j] = pack2h(lo_f(v[j]) * usc, hi_f(v[j]) * usc);
    int c = pl + 128 * i, row = (c >> 2) + 31;
    *(u32x4*)(cbp + CV_UIMG + row * 64 + (((c & 3) ^ ((row >> 2) & 3)) << 4)) = o;
  }
  __syncthreads();

  f32x16 y0, y1;
#pragma unroll
  for (int i = 0; i < 16; ++i) y0[i] = y1[i] = 0.f;
  const unsigned cbase = lds_addr(cbp);
  const int rho = (2047 + 8 * h - l31) & 3;
  unsigned aaddr = cbase + CV_COPY + rho * CV_CSTR + (2047 + 16 * hw + 8 * h - l31 - rho) * 2;
  const int chk = 2 * hw + h;
  int row = l31 + 31;
  s16x4 al[3], ah[3];
  half8 b0[3], b1[3];
#define CV_RD2(slot) { \
      unsigned r0_ = cbase + CV_UIMG + row * 64 + ((chk ^ ((row >> 2) & 3)) << 4); \
      DS_B64(al[slot], aaddr, 0); DS_B64(ah[slot], aaddr, 8); DS_B128(b0[slot], r0_, 0); DS_B128(b1[slot], r0_, 2048); \
      aaddr -= 64; row -= 1; }
#define CV_RD1(slot) { \
      unsigned r0_ = cbase + CV_UIMG + row * 64 + ((chk ^ ((row >> 2) & 3)) << 4); \
      DS_B64(al[slot], aaddr, 0); DS_B64(ah[slot], aaddr, 8); DS_B128(b1[slot], r0_, 2048); \
      aaddr -= 64; row -= 1; }
#define CV_MM2(slot) { half8 af_ = cat_h(al[slot], ah[slot]); y0 = MFMA_H(af_, b0[slot], y0); y1 = MFMA_H(af_, b1[slot], y1); }
#define CV_MM1(slot) { y1 = MFMA_H(cat_h(al[slot], ah[slot]), b1[slot], y1); }
  CV_RD2(0) CV_RD2(1)
#pragma unroll 1
  for (int mm = 0; mm < 30; mm += 3) {
    CV_RD2(2) LGKM(8); CV_MM2(0) SCHED();
    CV_RD2(0) LGKM(8); CV_MM2(1) SCHED();
    CV_RD2(1) LGKM(8); CV_MM2(2) SCHED();
  }
  CV_RD1(2) LGKM(7); CV_MM2(0) SCHED();
  CV_RD1(0) LGKM(6); CV_MM2(1) SCHED();
#pragma unroll 1
  for (int mm = 32; mm < 62; mm += 3) {
    CV_RD1(1) LGKM(6); CV_MM1(2) SCHED();
    CV_RD1(2) LGKM(6); CV_MM1(0) SCHED();
    CV_RD1(0) LGKM(6); CV_MM1(1) SCHED();
  }
  LGKM(3); CV_MM1(2) SCHED();
  LGKM(0); CV_MM1(0) SCHED();

  float* xch = (float*)(cbp + CV_COPY);
  __syncthreads();
  {
    f32x16& snd = hw == 0 ? y1 : y0;
#pragma unroll
    for (int g = 0; g < 4; ++g)
      *(f32x4*)(xch + hw * 1024 + g * 256 + lane * 4) = (f32x4){snd[4 * g], snd[4 * g + 1], snd[4 * g + 2], snd[4 * g + 3]};
  }
  __syncthreads();
  f32x16 ya = hw == 0 ? y0 : y1;
#pragma unroll
  for (int g = 0; g < 4; ++g) {
    f32x4 v = *(const f32x4*)(xch + (hw ^ 1) * 1024 + g * 256 + lane * 4);
#pragma unroll
    for (int e = 0; e < 4; ++e) ya[4 * g + e] += v[e];
  }
  const float osc = kinv / usc;
  u16* yd = (u16*)(ws + WS_YT) + (size_t)d * L_ + 1024 * hw;
#pragma unroll
  for (int g = 0; g < 4; ++g) {
    u32x2 v;
    v[0] = pack2bf(ya[4 * g] * osc, ya[4 * g + 1] * osc);
    v[1] = pack2bf(ya[4 * g + 2] * osc, ya[4 * g + 3] * osc);
    *(u32x2*)(yd + 32 * l31 + 8 * g + 4 * h) = v;
  }

}

__global__ __launch_bounds__(256, 3) void kernelM(const u16* __restrict__ U, const u16* __restrict__ gw,
                                                    const u16* __restrict__ Wo, char* __restrict__ ws) {
  extern __shared__ __attribute__((aligned(16))) char smem[];
  const int tid = threadIdx.x, lane = tid & 63, wave = tid >> 6;
  int b = blockIdx.x;
  if (b < 512) { conv_block(ws, smem, b); return; }
  b -= 512;
  if (b >= 256) {
    b -= 256;
    float* G = (float*)(ws + WS_G);
    u32x4 w0 = *(const u32x4*)(gw + lane * 16), w1 = *(const u32x4*)(gw + lane * 16 + 8);
    u32x4 xa[4], xb[4];
#pragma unroll
    for (int i = 0; i < 4; ++i) {
      int row = b * 16 + wave * 4 + i;
      xa[i] = *(const u32x4*)(U + (size_t)row * D_ + lane * 16);
      xb[i] = *(const u32x4*)(U + (size_t)row * D_ + lane * 16 + 8);
    }
#pragma unroll
    for (int i = 0; i < 4; ++i) {
      int row = b * 16 + wave * 4 + i;
      u32x4 x0 = xa[i], x1 = xb[i];
      float s = 0.f;
#pragma unroll
      for (int j = 0; j < 4; ++j) {
        unsigned p0 = pack2bf(lo_f(x0[j]) * lo_f(w0[j]), hi_f(x0[j]) * hi_f(w0[j]));
        unsigned p1 = pack2bf(lo_f(x1[j]) * lo_f(w1[j]), hi_f(x1[j]) * hi_f(w1[j]));
        s += (lo_f(p0) + hi_f(p0)) + (lo_f(p1) + hi_f(p1));
      }
      s = bfr(wave_sum(s));
      float g = bfr(1.0f / (1.0f + __expf(-s)));
      if (lane == 0) G[row] = g;
    }
    {
      u32x4 t = {0, 0, 0, 0};
#pragma unroll
      for (int i = 0; i < 4; ++i) t ^= *(const u32x4*)(Wo + (size_t)b * 8192 + i * 2048 + tid * 8);
      asm volatile("" :: "v"(t));
    }
    return;
  }
  u16* kvT = (u16*)smem;
  float* zs = (float*)(smem + 2560);
  float* zpart = (float*)(smem + 4096);
  const int head = b >> 3, chunk = b & 7;
  const int l31 = lane & 31, h = lane >> 5;
  const float* kvp = (const float*)(ws + WS_KVP);
  const float* zp = (const float*)(ws + WS_ZP);
  const u16* pq = (const u16*)(ws + WS_PQ);
  u16* o = (u16*)(ws + WS_O);
  u32x4 qv[2][2];
#pragma unroll
  for (int rt = 0; rt < 2; ++rt)
#pragma unroll
    for (int ks = 0; ks < 2; ++ks)
      qv[rt][ks] = *(const u32x4*)(pq + (size_t)(chunk * 256 + wave * 64 + rt * 32 + l31) * D_ + head * 32 + 16 * ks + 8 * h);
  {
    int e = tid >> 3, d4 = (tid & 7) * 4;
    f32x4 p[16];
#pragma unroll
    for (int r = 0; r < 16; ++r) p[r] = *(const f32x4*)(kvp + ((size_t)(r * 32 + head) * 32 + e) * 32 + d4);
    float zz0 = zp[((tid >> 5) * 32 + head) * 32 + (tid & 31)];
    float zz1 = zp[((8 + (tid >> 5)) * 32 + head) * 32 + (tid & 31)];
    f32x4 s = p[0];
#pragma unroll
    for (int r = 1; r < 16; ++r) s += p[r];
    u32x2 v;
    v[0] = pack2bf(s[0], s[1]); v[1] = pack2bf(s[2], s[3]);
    *(u32x2*)(kvT + e * 40 + d4) = v;
    zpart[tid] = zz0 + zz1;
    __syncthreads();
    if (tid < 32) {
      float z = 0.f;
#pragma unroll
      for (int r = 0; r < 8; ++r) z += zpart[r * 32 + tid];
      zs[tid] = bfr(z);
    }
  }
  __syncthreads();
  bf16x8 ka[2];
  float zr[2][8];
#pragma unroll
  for (int ks = 0; ks < 2; ++ks) {
    ka[ks] = *(const bf16x8*)(kvT + l31 * 40 + 16 * ks + 8 * h);
#pragma unroll
    for (int j = 0; j < 8; ++j) zr[ks][j] = zs[16 * ks + 8 * h + j];
  }
#pragma unroll
  for (int rt = 0; rt < 2; ++rt) {
    int s = chunk * 256 + wave * 64 + rt * 32 + l31;
    f32x16 acc;
#pragma unroll
    for (int i = 0; i < 16; ++i) acc[i] = 0.f;
    float den = 0.f;
#pragma unroll
    for (int ks = 0; ks < 2; ++ks) {
      u32x4 q = qv[rt][ks];
#pragma unroll
      for (int j = 0; j < 4; ++j) {
        den += lo_f(q[j]) * zr[ks][2 * j];
        den += hi_f(q[j]) * zr[ks][2 * j + 1];
      }
      acc = MFMA_BF(ka[ks], __builtin_bit_cast(bf16x8, q), acc);
    }
    den += __shfl_xor(den, 32);
    den = bfr(bfr(den) + 1e-6f);
    float rd = 1.0f / den;
#pragma unroll
    for (int g = 0; g < 4; ++g) {
      unsigned n0 = pack2bf(acc[4 * g], acc[4 * g + 1]), n1 = pack2bf(acc[4 * g + 2], acc[4 * g + 3]);
      u32x2 v;
      v[0] = pack2bf(lo_f(n0) * rd, hi_f(n0) * rd);
      v[1] = pack2bf(lo_f(n1) * rd, hi_f(n1) * rd);
      *(u32x2*)(o + (size_t)s * D_ + head * 32 + 8 * g + 4 * h) = v;
    }
  }
}

constexpr int B_STAGE = 24576, B_NST = 6;
__global__ __launch_bounds__(512) void kernelB(const u16* __restrict__ U, const u16* __restrict__ Dskip,
                                                 const u16* __restrict__ Wo, const char* __restrict__ ws, u16* __restrict__ out) {
  extern __shared__ __attribute__((aligned(16))) char smem[];
  const int b = blockIdx.x, xcd = b & 7, idx = b >> 3;
  const int rb = (xcd >> 1) * 4 + (idx & 3);
  const int cbk = (xcd & 1) * 8 + (idx >> 2);
  const int tid = threadIdx.x, lane = tid & 63;
  const int wave = __builtin_amdgcn_readfirstlane(tid >> 6);
  const int wm = wave & 3, wn = wave >> 2;
  const int l31 = lane & 31, h = lane >> 5;
  const u16* O = (const u16*)(ws + WS_O);

  const u16* ga[2];
  const u16* gb;
#pragma unroll
  for (int i = 0; i < 2; ++i) {
    int row = 16 * wave + 8 * i + (lane >> 3);
    int chunk = (lane & 7) ^ ((row >> 1) & 7);
    ga[i] = O + (size_t)(rb * 128 + row) * D_ + chunk * 8;
  }
  {
    int kr = 8 * wave + (lane >> 3);
    int chunk = (lane & 7) ^ (((kr >> 1) & 1) << 2);
    gb = Wo + (size_t)kr * D_ + cbk * 64 + chunk * 8;
  }
  auto stage = [&](int kt, int buf) {
    char* base = smem + buf * B_STAGE;
    glds16(ga[0] + kt * 64, base + (16 * wave) * 128);
    glds16(ga[1] + kt * 64, base + (16 * wave + 8) * 128);
    glds16(gb + (size_t)kt * 64 * D_, base + 16384 + wave * 1024);
  };
  const unsigned sb = lds_addr(smem);
  unsigned aad[4];
  {
    int row = 32 * wm + l31;
#pragma unroll
    for (int ks = 0; ks < 4; ++ks) aad[ks] = sb + row * 128 + (((2 * ks + h) ^ ((row >> 1) & 7)) << 4);
  }
  const int blk = (lane >> 4) & 1, q4 = (lane & 15) >> 2, p4 = lane & 3;
  const unsigned bad = sb + 16384 + (8 * h + q4) * 128 + ((wn ^ (q4 >> 1)) << 6) + blk * 32 + p4 * 8;
  f32x16 acc;
#pragma unroll
  for (int i = 0; i < 16; ++i) acc[i] = 0.f;
#pragma unroll
  for (int s = 0; s < 6; ++s) stage(s, s);
  const int dcol = cbk * 64 + wn * 32 + l31;
  const int trow = rb * 128 + 32 * wm + 4 * h;
  const float dsk = bf2f(Dskip[dcol]);
  const float* G = (const float*)(ws + WS_G);
  const u16* yT = (const u16*)(ws + WS_YT) + (size_t)dcol * L_;
  u32x2 yv[4]; f32x4 gv[4]; u16 uu[16];
#pragma unroll
  for (int g = 0; g < 4; ++g) {
    yv[g] = *(const u32x2*)(yT + trow + 8 * g);
    gv[g] = *(const f32x4*)(G + trow + 8 * g);
#pragma unroll
    for (int e = 0; e < 4; ++e) uu[4 * g + e] = U[(size_t)(trow + 8 * g + e) * D_ + dcol];
  }
  unsigned aadh[4], aadh2[4];
#pragma unroll
  for (int ks = 0; ks < 4; ++ks) { aadh[ks] = aad[ks] + 2 * B_STAGE; aadh2[ks] = aad[ks] + 4 * B_STAGE; }
  const unsigned badh = bad + 2 * B_STAGE, badh2 = bad + 4 * B_STAGE;
  bf16x8 a[2][4]; s16x4 bl[2][4], bh[2][4];
#define B_RD2(S, ks, AA, BB, bo) DS_B128(a[S][ks], AA[ks], bo); DS_TR(bl[S][ks], BB, bo + ks * 2048); DS_TR(bh[S][ks], BB, bo + ks * 2048 + 512);
#define B_RD(S, ks, buf) if constexpr ((buf) < 2) { B_RD2(S, ks, aad, bad, (buf) * B_STAGE) } else if constexpr ((buf) < 4) { B_RD2(S, ks, aadh, badh, ((buf) - 2) * B_STAGE) } else { B_RD2(S, ks, aadh2, badh2, ((buf) - 4) * B_STAGE) }
#define B_MM(S, ks) { acc = MFMA_BF(a[S][ks], cat_bf(bl[S][ks], bh[S][ks]), acc); }
#define B_STEP(kt, buf, S, WAITN) { \
    asm volatile("s_waitcnt vmcnt(" #WAITN ") lgkmcnt(0)" ::: "memory"); \
    __builtin_amdgcn_s_barrier(); \
 \
    if ((kt) + 6 < 16) stage((kt) + 6, buf); \
    if ((kt) < 15) { B_RD((S) ^ 1, 0, ((buf) + 1) % 6) B_RD((S) ^ 1, 1, ((buf) + 1) % 6) } \
    SCHED(); B_MM(S, 0) B_MM(S, 1) SCHED(); \
    if ((kt) < 15) { B_RD((S) ^ 1, 2, ((buf) + 1) % 6) B_RD((S) ^ 1, 3, ((buf) + 1) % 6) } \
    SCHED(); B_MM(S, 2) B_MM(S, 3) asm volatile("" : "+v"(acc)); SCHED(); }
  asm volatile("s_waitcnt vmcnt(40)" ::: "memory");
  __builtin_amdgcn_s_barrier();

  B_RD(0, 0, 0) B_RD(0, 1, 0) B_RD(0, 2, 0) B_RD(0, 3, 0)
  B_STEP(0, 0, 0, 37) B_STEP(1, 1, 1, 37) B_STEP(2, 2, 0, 37) B_STEP(3, 3, 1, 37) B_STEP(4, 4, 0, 37)
  B_STEP(5, 5, 1, 12) B_STEP(6, 0, 0, 12) B_STEP(7, 1, 1, 12) B_STEP(8, 2, 0, 12) B_STEP(9, 3, 1, 12) B_STEP(10, 4, 0, 12)
  B_STEP(11, 5, 1, 9) B_STEP(12, 0, 0, 6) B_STEP(13, 1, 1, 3) B_STEP(14, 2, 0, 0) B_STEP(15, 3, 1, 0)
#pragma unroll
  for (int g = 0; g < 4; ++g) {
#pragma unroll
    for (int e2 = 0; e2 < 2; ++e2) {
      unsigned ud = pack2bf(bf2f(uu[4 * g + 2 * e2]) * dsk, bf2f(uu[4 * g + 2 * e2 + 1]) * dsk);
      unsigned a = pack2bf(lo_f(yv[g][e2]) + lo_f(ud), hi_f(yv[g][e2]) + hi_f(ud));
      unsigned bb = pack2bf(acc[4 * g + 2 * e2], acc[4 * g + 2 * e2 + 1]);
      float g0 = gv[g][2 * e2], g1 = gv[g][2 * e2 + 1];
      unsigned og = pack2bf(1.0f - g0, 1.0f - g1);
      unsigned ga_ = pack2bf(g0 * lo_f(a), g1 * hi_f(a));
      unsigned gb_ = pack2bf(lo_f(og) * lo_f(bb), hi_f(og) * hi_f(bb));
      unsigned r = pack2bf(lo_f(ga_) + lo_f(gb_), hi_f(ga_) + hi_f(gb_));
      int t = trow + 8 * g + 2 * e2;
      out[(size_t)t * D_ + dcol] = (u16)(r & 0xffffu);
      out[(size_t)(t + 1) * D_ + dcol] = (u16)(r >> 16);
    }
  }

}

extern "C" void kernel_launch(void* const* d_in, const int* in_sizes, int n_in, void* d_out, int out_size, void* d_ws,
                              size_t ws_size, hipStream_t stream) {
  const u16* U = (const u16*)d_in[0];
  char* ws = (char*)d_ws;
  (void)hipFuncSetAttribute((const void*)kernelA, hipFuncAttributeMaxDynamicSharedMemorySize, A_LDS);
  (void)hipFuncSetAttribute((const void*)kernelM, hipFuncAttributeMaxDynamicSharedMemorySize, CV_LDS);
  (void)hipFuncSetAttribute((const void*)kernelB, hipFuncAttributeMaxDynamicSharedMemorySize, B_STAGE * B_NST);
  kernelA<<<512, 512, A_LDS, stream>>>(U, (const u16*)d_in[1], (const u16*)d_in[2], (const u16*)d_in[3],
                                       (const u16*)d_in[4], (const u16*)d_in[5], (const u16*)d_in[6],
                                       (const u16*)d_in[7], (const u16*)d_in[10], ws);
  kernelM<<<512 + 256 + 128, 256, CV_LDS, stream>>>(U, (const u16*)d_in[12], (const u16*)d_in[11], ws);
  kernelB<<<256, 512, B_STAGE * B_NST, stream>>>(U, (const u16*)d_in[8], (const u16*)d_in[11], ws, (u16*)d_out);
}
```

```cpp
#include <hip/hip_runtime.h>
#include <hip/hip_bf16.h>
#include <stdint.h>

typedef __attribute__((ext_vector_type(8))) __bf16 bf16x8;
typedef __attribute__((ext_vector_type(8))) _Float16 half8;
typedef __attribute__((ext_vector_type(4))) _Float16 half4;
typedef __attribute__((ext_vector_type(2))) _Float16 half2v;
typedef __attribute__((ext_vector_type(4))) short s16x4;
typedef __attribute__((ext_vector_type(8))) short s16x8;
typedef __attribute__((ext_vector_type(16))) float f32x16;
typedef __attribute__((ext_vector_type(4))) float f32x4;
typedef __attribute__((ext_vector_type(2))) float f32x2;
typedef __attribute__((ext_vector_type(4))) unsigned u32x4;
typedef __attribute__((ext_vector_type(2))) unsigned u32x2;
typedef unsigned short u16;

#define DI __device__ __forceinline__
#define LDS_AS __attribute__((address_space(3)))

constexpr int L_ = 2048, D_ = 1024, NST = 128;

DI float bf2f(u16 x) { return __uint_as_float(((unsigned)x) << 16); }
DI u16 f2bf(float x) { unsigned u = __float_as_uint(x); u += 0x7fffu + ((u >> 16) & 1u); return (u16)(u >> 16); }
DI float bfr(float x) { unsigned u = __float_as_uint(x); u += 0x7fffu + ((u >> 16) & 1u); return __uint_as_float(u & 0xffff0000u); }
typedef __attribute__((ext_vector_type(2))) __bf16 bf16x2v;
DI unsigned pack2bf(float lo, float hi) { f32x2 v = {lo, hi}; return __builtin_bit_cast(unsigned, __builtin_convertvector(v, bf16x2v)); }
DI float lo_f(unsigned p) { return __uint_as_float(p << 16); }
DI float hi_f(unsigned p) { return __uint_as_float(p & 0xffff0000u); }
DI unsigned pack2h(float lo, float hi) { f32x2 v = {lo, hi}; return __builtin_bit_cast(unsigned, __builtin_convertvector(v, half2v)); }

DI void glds16(const void* g, char* l) {
  __builtin_amdgcn_global_load_lds((const __attribute__((address_space(1))) void*)g, (LDS_AS void*)l, 16, 0, 0);
}
DI bf16x8 cat_bf(s16x4 a, s16x4 b) {
  s16x8 r = __builtin_shufflevector(a, b, 0, 1, 2, 3, 4, 5, 6, 7);
  return __builtin_bit_cast(bf16x8, r);
}
DI half8 cat_h(s16x4 a, s16x4 b) {
  s16x8 r = __builtin_shufflevector(a, b, 0, 1, 2, 3, 4, 5, 6, 7);
  return __builtin_bit_cast(half8, r);
}
DI unsigned lds_addr(const char* p) { return (unsigned)(size_t)(LDS_AS const char*)p; }
#define DS_B128(dst, addr, off) asm volatile("ds_read_b128 %0, %1 offset:%2" : "=v"(dst) : "v"(addr), "i"(off) : "memory")
#define DS_B64(dst, addr, off) asm volatile("ds_read_b64 %0, %1 offset:%2" : "=v"(dst) : "v"(addr), "i"(off) : "memory")
#define DS_TR(dst, addr, off) asm volatile("ds_read_b64_tr_b16 %0, %1 offset:%2" : "=v"(dst) : "v"(addr), "i"(off) : "memory")
#define LGKM(n) do { asm volatile("s_waitcnt lgkmcnt(" #n ")" ::: "memory"); __builtin_amdgcn_sched_barrier(0); } while (0)
#define SCHED() __builtin_amdgcn_sched_barrier(0)
#define MFMA_BF(a, b, c) __builtin_amdgcn_mfma_f32_32x32x16_bf16((a), (b), (c), 0, 0, 0)
#define MFMA_H(a, b, c) __builtin_amdgcn_mfma_f32_32x32x16_f16((a), (b), (c), 0, 0, 0)

DI unsigned phi2(float a0, float a1) {
  unsigned t = pack2bf(a0, a1);
  float t0 = lo_f(t), t1 = hi_f(t);
  float r0 = t0 > 0.f ? t0 + 1.0f : __builtin_amdgcn_exp2f(t0 * 1.44269504088896f);
  float r1 = t1 > 0.f ? t1 + 1.0f : __builtin_amdgcn_exp2f(t1 * 1.44269504088896f);
  unsigned r = pack2bf(r0, r1);
  return pack2bf(lo_f(r) + 1e-6f, hi_f(r) + 1e-6f);
}

constexpr size_t WS_PQ = 0;
constexpr size_t WS_O = WS_PQ + (size_t)L_ * D_ * 2;
constexpr size_t WS_YT = WS_O + (size_t)L_ * D_ * 2;
constexpr size_t WS_KVP = WS_YT + (size_t)L_ * D_ * 2;
constexpr size_t WS_ZP = WS_KVP + (size_t)16 * 32 * 1024 * 4;
constexpr size_t WS_G = WS_ZP + (size_t)16 * 32 * 32 * 4;
constexpr size_t WS_UT = WS_G + 8192;
constexpr size_t WS_KG = WS_UT + (size_t)L_ * D_ * 2;
constexpr size_t WS_KS = WS_KG + (size_t)D_ * 1296;
constexpr int A_LDS = 80 * 1024;

constexpr int G1_STAGE = 20480;
DI void gemm1_block(const u16* __restrict__ X, const u16* __restrict__ W, u16* __restrict__ pq, float* __restrict__ kvp,
                    float* __restrict__ zp, char* smem, int b) {

  const int xcd = b & 7, idx = b >> 3;
  const int rb = (xcd >> 2) * 8 + (idx & 7);
  const int hp = (xcd & 3) * 4 + (idx >> 3);
  const int tid = threadIdx.x, lane = tid & 63;
  const int wave = __builtin_amdgcn_readfirstlane(tid >> 6);
  const int wm = wave & 3, hh = wave >> 2;
  const int l31 = lane & 31, h = lane >> 5;
  const u16* ga;
  {
    int row = 16 * wave + (lane >> 2);
    int chunk = (lane & 3) ^ ((row >> 2) & 3);
    ga = X + (size_t)(rb * 128 + row) * D_ + chunk * 8;
  }
  const u16* gbl;
  {
    int kr = lane >> 3;
    int chunk = (lane & 7) ^ (((kr >> 1) & 1) << 2);
    gbl = W + (size_t)kr * 3072 + hp * 64 + chunk * 8;
  }
  auto stage = [&](int kt) {
    char* base = smem + (kt & 3) * G1_STAGE;
    glds16(ga + kt * 32, base + wave * 1024);
    int j0 = (wave + 4 * kt) & 7;
    {
      int slab = j0 >> 2, part = j0 & 3;
      glds16(gbl + (size_t)(kt * 32 + part * 8) * 3072 + slab * 1024, base + 8192 + j0 * 1024);
    }
    if (j0 < 4) glds16(gbl + (size_t)(kt * 32 + j0 * 8) * 3072 + 2048, base + 8192 + (8 + j0) * 1024);
  };
  const unsigned sb = lds_addr(smem);
  unsigned aad[2];
  {
    int row = 32 * wm + l31;
#pragma unroll
    for (int ks = 0; ks < 2; ++ks) aad[ks] = sb + row * 64 + (((2 * ks + h) ^ ((row >> 2) & 3)) << 4);
  }
  const int blk = (lane >> 4) & 1, q4 = (lane & 15) >> 2, p4 = lane & 3;
  const unsigned bad = sb + 8192 + (8 * h + q4) * 128 + ((hh ^ (q4 >> 1)) << 6) + blk * 32 + p4 * 8;

  f32x16 accq, acck, accv;
#pragma unroll
  for (int i = 0; i < 16; ++i) accq[i] = acck[i] = accv[i] = 0.f;

  const unsigned aadh[2] = {aad[0] + 2 * G1_STAGE, aad[1] + 2 * G1_STAGE};
  const unsigned badh = bad + 2 * G1_STAGE;
  bf16x8 a[2]; s16x4 bl[2][3], bh[2][3];
#define G1_RD2(ks, AA, BB, bo) \
  DS_B128(a[ks], AA[ks], bo); \
  DS_TR(bl[ks][0], BB, bo + ks * 2048); DS_TR(bh[ks][0], BB, bo + ks * 2048 + 512); \
  DS_TR(bl[ks][1], BB, bo + ks * 2048 + 4096); DS_TR(bh[ks][1], BB, bo + ks * 2048 + 4096 + 512); \
  DS_TR(bl[ks][2], BB, bo + ks * 2048 + 8192); DS_TR(bh[ks][2], BB, bo + ks * 2048 + 8192 + 512);
#define G1_RD(ks, buf) if constexpr ((buf) < 2) { G1_RD2(ks, aad, bad, (buf) * G1_STAGE) } else { G1_RD2(ks, aadh, badh, ((buf) - 2) * G1_STAGE) }
#define G1_MM(ks) { \
  accq = MFMA_BF(cat_bf(bl[ks][0], bh[ks][0]), a[ks], accq); \
  acck = MFMA_BF(a[ks], cat_bf(bl[ks][1], bh[ks][1]), acck); \
  accv = MFMA_BF(a[ks], cat_bf(bl[ks][2], bh[ks][2]), accv); }
#define G1_STEP(kt, buf, WAITN, MORE) { \
    G1_RD(1, buf) \
    LGKM(7); G1_MM(0) SCHED(); \
    asm volatile("s_waitcnt vmcnt(" #WAITN ") lgkmcnt(0)" ::: "memory"); \
    __builtin_amdgcn_s_barrier(); \
    if ((kt) + 4 < 32) stage((kt) + 4); \
    if (MORE) { G1_RD(0, ((buf) + 1) & 3) } \
    SCHED(); G1_MM(1) asm volatile("" : "+v"(accq), "+v"(acck), "+v"(accv)); SCHED(); }
  stage(0); stage(1); stage(2); stage(3);
  asm volatile("s_waitcnt vmcnt(7)" ::: "memory");
  __builtin_amdgcn_s_barrier();
  G1_RD(0, 0)
#pragma unroll 1
  for (int kt = 0; kt < 28; kt += 4) {
    G1_STEP(kt, 0, 5, 1) G1_STEP(kt + 1, 1, 5, 1) G1_STEP(kt + 2, 2, 5, 1) G1_STEP(kt + 3, 3, 5, 1)
  }
  G1_STEP(28, 0, 5, 1) G1_STEP(29, 1, 0, 1) G1_STEP(30, 2, 0, 1) G1_STEP(31, 3, 0, 0)

  const int head = hp * 2 + hh;
  {
    int s = rb * 128 + 32 * wm + l31;
#pragma unroll
    for (int g = 0; g < 4; ++g) {
      u32x2 v;
      v[0] = phi2(accq[4 * g], accq[4 * g + 1]);
      v[1] = phi2(accq[4 * g + 2], accq[4 * g + 3]);
      *(u32x2*)(pq + (size_t)s * D_ + head * 32 + 8 * g + 4 * h) = v;
    }
  }
  f32x16 kvacc;
#pragma unroll
  for (int i = 0; i < 16; ++i) kvacc[i] = 0.f;
  float zsum = 0.f;
#pragma unroll
  for (int s = 0; s < 2; ++s) {
    u32x4 pa, pb;
#pragma unroll
    for (int j = 0; j < 4; ++j) {
      pa[j] = phi2(acck[8 * s + 2 * j], acck[8 * s + 2 * j + 1]);
      zsum += lo_f(pa[j]) + hi_f(pa[j]);
      pb[j] = pack2bf(accv[8 * s + 2 * j], accv[8 * s + 2 * j + 1]);
    }
    kvacc = MFMA_BF(__builtin_bit_cast(bf16x8, pa), __builtin_bit_cast(bf16x8, pb), kvacc);
  }
  zsum += __shfl_xor(zsum, 32);
  __syncthreads();
  float* red = (float*)smem + (hh * 3 + wm - 1) * 1088;
  if (wm != 0) {
#pragma unroll
    for (int g = 0; g < 4; ++g) {
      f32x4 v = {kvacc[4 * g], kvacc[4 * g + 1], kvacc[4 * g + 2], kvacc[4 * g + 3]};
      *(f32x4*)(red + g * 256 + lane * 4) = v;
    }
    if (lane < 32) red[1024 + lane] = zsum;
  }
  __syncthreads();
  if (wm == 0) {
    float* dst = kvp + ((size_t)(rb * 32 + head) * 32 + l31) * 32 + 4 * h;
    const float* r0 = (const float*)smem + hh * 3 * 1088;
#pragma unroll
    for (int g = 0; g < 4; ++g) {
      f32x4 v = {kvacc[4 * g], kvacc[4 * g + 1], kvacc[4 * g + 2], kvacc[4 * g + 3]};
#pragma unroll
      for (int w = 0; w < 3; ++w) v += *(const f32x4*)(r0 + w * 1088 + g * 256 + lane * 4);
      *(f32x4*)(dst + 8 * g) = v;
    }
    if (lane < 32) zp[(rb * 32 + head) * 32 + lane] = zsum + r0[1024 + lane] + r0[1088 + 1024 + lane] + r0[2 * 1088 + 1024 + lane];
  }

}

DI float wave_max(float v) {
#pragma unroll
  for (int o = 32; o > 0; o >>= 1) v = fmaxf(v, __shfl_xor(v, o));
  return v;
}
DI float wave_sum(float v) {
#pragma unroll
  for (int o = 32; o > 0; o >>= 1) v += __shfl_xor(v, o);
  return v;
}
DI float pow2_scale(float m) {
  int e = (int)((__float_as_uint(m) >> 23) & 0xff);
  int se = 127 + 13 - (e - 127);
  se = se < 1 ? 1 : (se > 254 ? 254 : se);
  return __uint_as_float((unsigned)se << 23);
}
DI float ex2(float x) { return __builtin_amdgcn_exp2f(x); }
DI float sin_rev(float x) { return __builtin_amdgcn_sinf(__builtin_amdgcn_fractf(x)); }
DI float cos_rev(float x) { return __builtin_amdgcn_cosf(__builtin_amdgcn_fractf(x)); }

constexpr int AX_PAR = 0;
constexpr int AX_XCH = 4160;
constexpr int AX_CH = 12352;
constexpr int AX_TR = 4 * AX_CH;

DI void aux_block(const u16* __restrict__ U, const u16* __restrict__ Are, const u16* __restrict__ Aim,
                  const u16* __restrict__ Bre, const u16* __restrict__ Bim, const u16* __restrict__ Cre,
                  const u16* __restrict__ Cim, const u16* __restrict__ logdt, char* __restrict__ ws, char* smem, int b) {

  const int tid = threadIdx.x, lane = tid & 63;
  const int wave = __builtin_amdgcn_readfirstlane(tid >> 6);
  const int ch = wave >> 1, hw = wave & 1;
  const int d = b * 4 + ch;
  const int l31 = lane & 31, h = lane >> 5;
  char* cbp = smem + ch * AX_CH;
  const int tr0 = (b >> 4) * 128, tc0 = (b & 15) * 64;
  u32x4 ut[2];
#pragma unroll
  for (int i = 0; i < 2; ++i) {
    int e = tid + 512 * i;
    ut[i] = *(const u32x4*)(U + (size_t)(tr0 + (e >> 3)) * D_ + tc0 + (e & 7) * 8);
  }
  const float dt = bfr(__expf(bf2f(logdt[d])));
  float pa[2], pb[2], pwr[2], pwi[2], wmax = 0.f;
#pragma unroll
  for (int i = 0; i < 2; ++i) {
    int n = lane + 64 * i;
    float are = bf2f(Are[d * NST + n]), aim = bf2f(Aim[d * NST + n]);
    float bre = dt * bf2f(Bre[d * NST + n]), bim = dt * bf2f(Bim[d * NST + n]);
    float cre = bf2f(Cre[d * NST + n]), cim = bf2f(Cim[d * NST + n]);
    pa[i] = dt * are * 1.44269504088896f;
    pb[i] = dt * aim * 0.159154943091895f;
    pwr[i] = cre * bre - cim * bim;
    pwi[i] = cre * bim + cim * bre;
    wmax = fmaxf(wmax, fmaxf(fabsf(pwr[i]), fabsf(pwi[i])));
  }
  wmax = wave_max(wmax);
  const float wsc = wmax > 0.f ? pow2_scale(wmax) : 1.f;
  float k2048 = 0.f;
  float* par = (float*)(cbp + AX_PAR);
#pragma unroll
  for (int i = 0; i < 2; ++i) {
    float wr = pwr[i] * wsc, wi = pwi[i] * wsc;
    float e1 = ex2(pa[i] * 1024.f), c1 = cos_rev(pb[i] * 1024.f), s1 = sin_rev(pb[i] * 1024.f);
    float er = e1 * c1, ei = e1 * s1;
    float e2r = er * er - ei * ei, e2i = 2.f * er * ei;
    k2048 += wr * e2r - wi * e2i;
    if (i == hw) {
      int n = lane + 64 * i;
      *(f32x4*)(par + n * 8) = (f32x4){pa[i], pb[i], wr, wi};
      *(f32x4*)(par + n * 8 + 4) = (f32x4){er, ei, 0.f, 0.f};
    }
  }
  k2048 = wave_sum(k2048);

  f32x16 ka0, ka1;
#pragma unroll
  for (int i = 0; i < 16; ++i) ka0[i] = ka1[i] = 0.f;
  const float rf = (float)(32 * l31), cf = (float)l31;
  unsigned paddr = lds_addr(cbp + AX_PAR) + (64 * hw + 4 * h) * 32;
  f32x4 P0[2][4]; f32x2 P1[2][4];
#define KG_RD(S) { _Pragma("unroll") for (int jj = 0; jj < 4; ++jj) { DS_B128(P0[S][jj], paddr, jj * 32); DS_B64(P1[S][jj], paddr, jj * 32 + 16); } paddr += 256; }
#define KG_DO(S) { u32x4 fa0, fa1, fb; \
    _Pragma("unroll") for (int jj = 0; jj < 4; ++jj) { \
      f32x4 v0 = P0[S][jj]; f32x2 v1 = P1[S][jj]; \
      float ex = ex2(v0[0] * rf), ph = v0[1] * rf; \
      float cx = ex * cos_rev(ph), sx = ex * sin_rev(ph); \
      float xr = v0[2] * cx - v0[3] * sx, xi = v0[2] * sx + v0[3] * cx; \
      float x1r = xr * v1[0] - xi * v1[1], x1i = xr * v1[1] + xi * v1[0]; \
      float ey = ex2(v0[0] * cf), py = v0[1] * cf; \
      fa0[jj] = pack2h(xr, -xi); fa1[jj] = pack2h(x1r, -x1i); fb[jj] = pack2h(ey * cos_rev(py), ey * sin_rev(py)); } \
    ka0 = MFMA_H(__builtin_bit_cast(half8, fa0), __builtin_bit_cast(half8, fb), ka0); \
    ka1 = MFMA_H(__builtin_bit_cast(half8, fa1), __builtin_bit_cast(half8, fb), ka1); }
  asm volatile("s_waitcnt lgkmcnt(0)" ::: "memory");
  KG_RD(0)
#pragma unroll 1
  for (int t = 0; t < 8; t += 2) {
    KG_RD(1) LGKM(8); KG_DO(0) SCHED();
    if (t + 2 < 8) { KG_RD(0) LGKM(8); } else { LGKM(0); }
    KG_DO(1) SCHED();
  }

  float* xch = (float*)(cbp + AX_XCH);
  if (hw == 1) {
#pragma unroll
    for (int g = 0; g < 4; ++g) {
      *(f32x4*)(xch + g * 256 + lane * 4) = (f32x4){ka0[4 * g], ka0[4 * g + 1], ka0[4 * g + 2], ka0[4 * g + 3]};
      *(f32x4*)(xch + 1024 + g * 256 + lane * 4) = (f32x4){ka1[4 * g], ka1[4 * g + 1], ka1[4 * g + 2], ka1[4 * g + 3]};
    }
  }
#pragma unroll
  for (int i = 0; i < 2; ++i) {
    int e = tid + 512 * i, row = e >> 3, c8 = (e & 7) * 8;
#pragma unroll
    for (int j = 0; j < 4; ++j) {
      *(u16*)(smem + AX_TR + (c8 + 2 * j) * 272 + row * 2) = (u16)(ut[i][j] & 0xffffu);
      *(u16*)(smem + AX_TR + (c8 + 2 * j + 1) * 272 + row * 2) = (u16)(ut[i][j] >> 16);
    }
  }
  __syncthreads();
  {
    u16* uT = (u16*)(ws + WS_UT);
#pragma unroll
    for (int i = 0; i < 2; ++i) {
      int e = tid + 512 * i, c = e >> 4, t8 = (e & 15) * 8;
      u32x4 v = *(const u32x4*)(smem + AX_TR + c * 272 + t8 * 2);
      __builtin_nontemporal_store(v, (u32x4*)(uT + (size_t)(tc0 + c) * L_ + tr0 + t8));
    }
  }
  if (hw == 0) {
#pragma unroll
    for (int g = 0; g < 4; ++g) {
      f32x4 v0 = *(const f32x4*)(xch + g * 256 + lane * 4), v1 = *(const f32x4*)(xch + 1024 + g * 256 + lane * 4);
#pragma unroll
      for (int e = 0; e < 4; ++e) { ka0[4 * g + e] += v0[e]; ka1[4 * g + e] += v1[e]; }
    }
    float kmax = fabsf(k2048);
#pragma unroll
    for (int i = 0; i < 16; ++i) kmax = fmaxf(kmax, fmaxf(fabsf(ka0[i]), fabsf(ka1[i])));
    kmax = wave_max(kmax);
    const float ksc = kmax > 0.f ? pow2_scale(kmax) : 1.f;
    _Float16* lg = (_Float16*)(cbp + AX_PAR);
#pragma unroll
    for (int i = 0; i < 16; ++i) {
      int r = (i & 3) + 8 * (i >> 2) + 4 * h;
      lg[32 * r + l31] = (_Float16)(ka0[i] * ksc);
      lg[1024 + 32 * r + l31] = (_Float16)(ka1[i] * ksc);
    }
    if (lane == 0) lg[2048] = (_Float16)(k2048 * ksc);
    __builtin_amdgcn_wave_barrier();
    char* gdst = ws + WS_KG + (size_t)d * 1296;
    const u16* lgu = (const u16*)lg;
#pragma unroll
    for (int i = 0; i < 2; ++i) {
      int c0 = 8 * (lane + 64 * i);
      if (c0 < 648) {
        u32x4 o;
#pragma unroll
        for (int j = 0; j < 4; ++j) {
          unsigned w2[2];
#pragma unroll
          for (int e = 0; e < 2; ++e) {
            int c = c0 + 2 * j + e; c = c > 640 ? 640 : c;
            int sh = c < 256 ? 0 : (c >> 7) - 1;
            w2[e] = lgu[(c - 128 * sh) << sh];
          }
          o[j] = w2[0] | (w2[1] << 16);
        }
        *(u32x4*)(gdst + c0 * 2) = o;
      }
    }
    if (lane == 0) ((float*)(ws + WS_KS))[d] = 1.0f / (wsc * ksc);
  }

}

__global__ __launch_bounds__(512, 4) void kernelA(const u16* __restrict__ U, const u16* __restrict__ Are,
    const u16* __restrict__ Aim, const u16* __restrict__ Bre, const u16* __restrict__ Bim, const u16* __restrict__ Cre,
    const u16* __restrict__ Cim, const u16* __restrict__ logdt, const u16* __restrict__ Wqkv, char* __restrict__ ws) {
  extern __shared__ __attribute__((aligned(16))) char smem[];
  const int b = blockIdx.x;
  if (b < 256)
    gemm1_block(U, Wqkv, (u16*)(ws + WS_PQ), (float*)(ws + WS_KVP), (float*)(ws + WS_ZP), smem, b);
  else
    aux_block(U, Are, Aim, Bre, Bim, Cre, Cim, logdt, ws, smem, b - 256);
}

constexpr int CV_CSTR = 4160;
constexpr int CV_COPY = 0;
constexpr int CV_UIMG = 4 * CV_CSTR;
constexpr int CV_GRID = CV_UIMG;
constexpr int CV_CH = CV_UIMG + 6080 + 32;
constexpr int CV_LDS = 2 * CV_CH + 64;

DI void conv_block(char* __restrict__ ws, char* smem, int b) {

  const int tid = threadIdx.x, lane = tid & 63;
  const int wave = __builtin_amdgcn_readfirstlane(tid >> 6);
  const int ch = wave >> 1, hw = wave & 1;
  const int d = b * 2 + ch;
  const int l31 = lane & 31, h = lane >> 5;
  const int pl = lane + 64 * hw;
  char* cbp = smem + ch * CV_CH;
  float* shs = (float*)(smem + 2 * CV_CH);
  const u32x4* gsrc = (const u32x4*)(ws + WS_KG + (size_t)d * 1296);
  const u32x4* usrc = (const u32x4*)(ws + WS_UT + (size_t)d * 4096);
  u32x4 gv0 = gsrc[pl < 81 ? pl : 80];
  u32x4 uv0 = usrc[pl], uv1 = usrc[pl + 128];
  const float kinv = ((const float*)(ws + WS_KS))[d];
  if (pl < 81) *(u32x4*)(cbp + CV_GRID + pl * 16) = gv0;
  {
    unsigned mx = 0;
#pragma unroll
    for (int j = 0; j < 4; ++j) {
      mx = max(mx, max(max(uv0[j] & 0x7fffu, (uv0[j] >> 16) & 0x7fffu), max(uv1[j] & 0x7fffu, (uv1[j] >> 16) & 0x7fffu)));
    }
    float m = wave_max(bf2f((u16)mx));
    if (lane == 0) shs[wave] = m;
  }
  __syncthreads();

  const u16* gridp = (const u16*)(cbp + CV_GRID);
  {
    unsigned v[2][11];
#pragma unroll
    for (int it = 0; it < 2; ++it)
#pragma unroll
      for (int e = 0; e < 11; ++e) {
        int lag = 2047 - (8 * pl + 1024 * it) - e;
        int lc = lag < 0 ? 0 : lag;
        int sh = 24 - __builtin_clz(lc | 128);
        int half_ = (1 << sh) >> 1;
        int ci = sh == 0 ? lc : 128 * sh + ((lc + half_ - 1 + ((lc >> sh) & 1)) >> sh);
        unsigned x = gridp[ci];
        v[it][e] = lag < 0 ? 0u : x;
      }
#pragma unroll
    for (int it = 0; it < 2; ++it)
#pragma unroll
      for (int rho = 0; rho < 4; ++rho) {
        u32x4 o;
#pragma unroll
        for (int j = 0; j < 4; ++j) o[j] = v[it][rho + 2 * j] | (v[it][rho + 2 * j + 1] << 16);
        *(u32x4*)(cbp + CV_COPY + rho * CV_CSTR + (8 * pl + 1024 * it) * 2) = o;
      }
    if (pl < 16) *(u32x4*)(cbp + CV_COPY + (pl >> 2) * CV_CSTR + (2048 + 8 * (pl & 3)) * 2) = (u32x4){0, 0, 0, 0};
  }
  __syncthreads();
  const float um = fmaxf(shs[2 * ch], shs[2 * ch + 1]);
  const float usc = um > 0.f ? pow2_scale(um) : 1.f;
  if (pl < 124) *(u32x4*)(cbp + CV_UIMG + pl * 16) = (u32x4){0, 0, 0, 0};
#pragma unroll
  for (int i = 0; i < 2; ++i) {
    u32x4 v = i ? uv1 : uv0, o;
#pragma unroll
    for (int j = 0; j < 4; ++j) o[j] = pack2h(lo_f(v[j]) * usc, hi_f(v[j]) * usc);
    int c = pl + 128 * i, row = (c >> 2) + 31;
    *(u32x4*)(cbp + CV_UIMG + row * 64 + (((c & 3) ^ ((row >> 2) & 3)) << 4)) = o;
  }
  __syncthreads();

  f32x16 y0, y1;
#pragma unroll
  for (int i = 0; i < 16; ++i) y0[i] = y1[i] = 0.f;
  const unsigned cbase = lds_addr(cbp);
  const int rho = (2047 + 8 * h - l31) & 3;
  unsigned aaddr = cbase + CV_COPY + rho * CV_CSTR + (2047 + 16 * hw + 8 * h - l31 - rho) * 2;
  const int chk = 2 * hw + h;
  int row = l31 + 31;
  s16x4 al[3], ah[3];
  half8 b0[3], b1[3];
#define CV_RD2(slot) { \
      unsigned r0_ = cbase + CV_UIMG + row * 64 + ((chk ^ ((row >> 2) & 3)) << 4); \
      DS_B64(al[slot], aaddr, 0); DS_B64(ah[slot], aaddr, 8); DS_B128(b0[slot], r0_, 0); DS_B128(b1[slot], r0_, 2048); \
      aaddr -= 64; row -= 1; }
#define CV_RD1(slot) { \
      unsigned r0_ = cbase + CV_UIMG + row * 64 + ((chk ^ ((row >> 2) & 3)) << 4); \
      DS_B64(al[slot], aaddr, 0); DS_B64(ah[slot], aaddr, 8); DS_B128(b1[slot], r0_, 2048); \
      aaddr -= 64; row -= 1; }
#define CV_MM2(slot) { half8 af_ = cat_h(al[slot], ah[slot]); y0 = MFMA_H(af_, b0[slot], y0); y1 = MFMA_H(af_, b1[slot], y1); }
#define CV_MM1(slot) { y1 = MFMA_H(cat_h(al[slot], ah[slot]), b1[slot], y1); }
  CV_RD2(0) CV_RD2(1)
#pragma unroll 1
  for (int mm = 0; mm < 30; mm += 3) {
    CV_RD2(2) LGKM(8); CV_MM2(0) SCHED();
    CV_RD2(0) LGKM(8); CV_MM2(1) SCHED();
    CV_RD2(1) LGKM(8); CV_MM2(2) SCHED();
  }
  CV_RD1(2) LGKM(7); CV_MM2(0) SCHED();
  CV_RD1(0) LGKM(6); CV_MM2(1) SCHED();
#pragma unroll 1
  for (int mm = 32; mm < 62; mm += 3) {
    CV_RD1(1) LGKM(6); CV_MM1(2) SCHED();
    CV_RD1(2) LGKM(6); CV_MM1(0) SCHED();
    CV_RD1(0) LGKM(6); CV_MM1(1) SCHED();
  }
  LGKM(3); CV_MM1(2) SCHED();
  LGKM(0); CV_MM1(0) SCHED();

  float* xch = (float*)(cbp + CV_COPY);
  __syncthreads();
  {
    f32x16& snd = hw == 0 ? y1 : y0;
#pragma unroll
    for (int g = 0; g < 4; ++g)
      *(f32x4*)(xch + hw * 1024 + g * 256 + lane * 4) = (f32x4){snd[4 * g], snd[4 * g + 1], snd[4 * g + 2], snd[4 * g + 3]};
  }
  __syncthreads();
  f32x16 ya = hw == 0 ? y0 : y1;
#pragma unroll
  for (int g = 0; g < 4; ++g) {
    f32x4 v = *(const f32x4*)(xch + (hw ^ 1) * 1024 + g * 256 + lane * 4);
#pragma unroll
    for (int e = 0; e < 4; ++e) ya[4 * g + e] += v[e];
  }
  const float osc = kinv / usc;
  u16* yd = (u16*)(ws + WS_YT) + (size_t)d * L_ + 1024 * hw;
#pragma unroll
  for (int g = 0; g < 4; ++g) {
    u32x2 v;
    v[0] = pack2bf(ya[4 * g] * osc, ya[4 * g + 1] * osc);
    v[1] = pack2bf(ya[4 * g + 2] * osc, ya[4 * g + 3] * osc);
    *(u32x2*)(yd + 32 * l31 + 8 * g + 4 * h) = v;
  }

}

__global__ __launch_bounds__(256, 3) void kernelM(const u16* __restrict__ U, const u16* __restrict__ gw,
                                                    const u16* __restrict__ Wo, char* __restrict__ ws) {
  extern __shared__ __attribute__((aligned(16))) char smem[];
  const int tid = threadIdx.x, lane = tid & 63, wave = tid >> 6;
  int b = blockIdx.x;
  if (b < 512) { conv_block(ws, smem, b); return; }
  b -= 512;
  if (b >= 256) {
    b -= 256;
    float* G = (float*)(ws + WS_G);
    u32x4 w0 = *(const u32x4*)(gw + lane * 16), w1 = *(const u32x4*)(gw + lane * 16 + 8);
    u32x4 xa[4], xb[4];
#pragma unroll
    for (int i = 0; i < 4; ++i) {
      int row = b * 16 + wave * 4 + i;
      xa[i] = *(const u32x4*)(U + (size_t)row * D_ + lane * 16);
      xb[i] = *(const u32x4*)(U + (size_t)row * D_ + lane * 16 + 8);
    }
#pragma unroll
    for (int i = 0; i < 4; ++i) {
      int row = b * 16 + wave * 4 + i;
      u32x4 x0 = xa[i], x1 = xb[i];
      float s = 0.f;
#pragma unroll
      for (int j = 0; j < 4; ++j) {
        unsigned p0 = pack2bf(lo_f(x0[j]) * lo_f(w0[j]), hi_f(x0[j]) * hi_f(w0[j]));
        unsigned p1 = pack2bf(lo_f(x1[j]) * lo_f(w1[j]), hi_f(x1[j]) * hi_f(w1[j]));
        s += (lo_f(p0) + hi_f(p0)) + (lo_f(p1) + hi_f(p1));
      }
      s = bfr(wave_sum(s));
      float g = bfr(1.0f / (1.0f + __expf(-s)));
      if (lane == 0) G[row] = g;
    }
    {
      u32x4 t = {0, 0, 0, 0};
#pragma unroll
      for (int i = 0; i < 4; ++i) t ^= *(const u32x4*)(Wo + (size_t)b * 8192 + i * 2048 + tid * 8);
      asm volatile("" :: "v"(t));
    }
    return;
  }
  u16* kvT = (u16*)smem;
  float* zs = (float*)(smem + 2560);
  float* zpart = (float*)(smem + 4096);
  const int head = b >> 3, chunk = b & 7;
  const int l31 = lane & 31, h = lane >> 5;
  const float* kvp = (const float*)(ws + WS_KVP);
  const float* zp = (const float*)(ws + WS_ZP);
  const u16* pq = (const u16*)(ws + WS_PQ);
  u16* o = (u16*)(ws + WS_O);
  u32x4 qv[2][2];
#pragma unroll
  for (int rt = 0; rt < 2; ++rt)
#pragma unroll
    for (int ks = 0; ks < 2; ++ks)
      qv[rt][ks] = *(const u32x4*)(pq + (size_t)(chunk * 256 + wave * 64 + rt * 32 + l31) * D_ + head * 32 + 16 * ks + 8 * h);
  {
    int e = tid >> 3, d4 = (tid & 7) * 4;
    f32x4 p[16];
#pragma unroll
    for (int r = 0; r < 16; ++r) p[r] = *(const f32x4*)(kvp + ((size_t)(r * 32 + head) * 32 + e) * 32 + d4);
    float zz0 = zp[((tid >> 5) * 32 + head) * 32 + (tid & 31)];
    float zz1 = zp[((8 + (tid >> 5)) * 32 + head) * 32 + (tid & 31)];
    f32x4 s = p[0];
#pragma unroll
    for (int r = 1; r < 16; ++r) s += p[r];
    u32x2 v;
    v[0] = pack2bf(s[0], s[1]); v[1] = pack2bf(s[2], s[3]);
    *(u32x2*)(kvT + e * 40 + d4) = v;
    zpart[tid] = zz0 + zz1;
    __syncthreads();
    if (tid < 32) {
      float z = 0.f;
#pragma unroll
      for (int r = 0; r < 8; ++r) z += zpart[r * 32 + tid];
      zs[tid] = bfr(z);
    }
  }
  __syncthreads();
  bf16x8 ka[2];
  float zr[2][8];
#pragma unroll
  for (int ks = 0; ks < 2; ++ks) {
    ka[ks] = *(const bf16x8*)(kvT + l31 * 40 + 16 * ks + 8 * h);
#pragma unroll
    for (int j = 0; j < 8; ++j) zr[ks][j] = zs[16 * ks + 8 * h + j];
  }
#pragma unroll
  for (int rt = 0; rt < 2; ++rt) {
    int s = chunk * 256 + wave * 64 + rt * 32 + l31;
    f32x16 acc;
#pragma unroll
    for (int i = 0; i < 16; ++i) acc[i] = 0.f;
    float den = 0.f;
#pragma unroll
    for (int ks = 0; ks < 2; ++ks) {
      u32x4 q = qv[rt][ks];
#pragma unroll
      for (int j = 0; j < 4; ++j) {
        den += lo_f(q[j]) * zr[ks][2 * j];
        den += hi_f(q[j]) * zr[ks][2 * j + 1];
      }
      acc = MFMA_BF(ka[ks], __builtin_bit_cast(bf16x8, q), acc);
    }
    den += __shfl_xor(den, 32);
    den = bfr(bfr(den) + 1e-6f);
    float rd = 1.0f / den;
#pragma unroll
    for (int g = 0; g < 4; ++g) {
      unsigned n0 = pack2bf(acc[4 * g], acc[4 * g + 1]), n1 = pack2bf(acc[4 * g + 2], acc[4 * g + 3]);
      u32x2 v;
      v[0] = pack2bf(lo_f(n0) * rd, hi_f(n0) * rd);
      v[1] = pack2bf(lo_f(n1) * rd, hi_f(n1) * rd);
      *(u32x2*)(o + (size_t)s * D_ + head * 32 + 8 * g + 4 * h) = v;
    }
  }
}

constexpr int B_STAGE = 24576, B_NST = 6;
__global__ __launch_bounds__(512) void kernelB(const u16* __restrict__ U, const u16* __restrict__ Dskip,
                                                 const u16* __restrict__ Wo, const char* __restrict__ ws, u16* __restrict__ out) {
  extern __shared__ __attribute__((aligned(16))) char smem[];
  const int b = blockIdx.x, xcd = b & 7, idx = b >> 3;
  const int rb = (xcd >> 1) * 4 + (idx & 3);
  const int cbk = (xcd & 1) * 8 + (idx >> 2);
  const int tid = threadIdx.x, lane = tid & 63;
  const int wave = __builtin_amdgcn_readfirstlane(tid >> 6);
  const int wm = wave & 3, wn = wave >> 2;
  const int l31 = lane & 31, h = lane >> 5;
  const u16* O = (const u16*)(ws + WS_O);

  const u16* ga[2];
  const u16* gb;
#pragma unroll
  for (int i = 0; i < 2; ++i) {
    int row = 16 * wave + 8 * i + (lane >> 3);
    int chunk = (lane & 7) ^ ((row >> 1) & 7);
    ga[i] = O + (size_t)(rb * 128 + row) * D_ + chunk * 8;
  }
  {
    int kr = 8 * wave + (lane >> 3);
    int chunk = (lane & 7) ^ (((kr >> 1) & 1) << 2);
    gb = Wo + (size_t)kr * D_ + cbk * 64 + chunk * 8;
  }
  auto stage = [&](int kt, int buf) {
    char* base = smem + buf * B_STAGE;
    glds16(ga[0] + kt * 64, base + (16 * wave) * 128);
    glds16(ga[1] + kt * 64, base + (16 * wave + 8) * 128);
    glds16(gb + (size_t)kt * 64 * D_, base + 16384 + wave * 1024);
  };
  const unsigned sb = lds_addr(smem);
  unsigned aad[4];
  {
    int row = 32 * wm + l31;
#pragma unroll
    for (int ks = 0; ks < 4; ++ks) aad[ks] = sb + row * 128 + (((2 * ks + h) ^ ((row >> 1) & 7)) << 4);
  }
  const int blk = (lane >> 4) & 1, q4 = (lane & 15) >> 2, p4 = lane & 3;
  const unsigned bad = sb + 16384 + (8 * h + q4) * 128 + ((wn ^ (q4 >> 1)) << 6) + blk * 32 + p4 * 8;
  f32x16 acc;
#pragma unroll
  for (int i = 0; i < 16; ++i) acc[i] = 0.f;
  const int dcol = cbk * 64 + wn * 32 + l31;
  const int trow = rb * 128 + 32 * wm + 4 * h;
  const float dsk = bf2f(Dskip[dcol]);
  const float* G = (const float*)(ws + WS_G);
  const u16* yT = (const u16*)(ws + WS_YT) + (size_t)dcol * L_;
  u32x2 yv[4]; f32x4 gv[4]; u16 uu[16];
#pragma unroll
  for (int g = 0; g < 4; ++g) {
    yv[g] = *(const u32x2*)(yT + trow + 8 * g);
    gv[g] = *(const f32x4*)(G + trow + 8 * g);
#pragma unroll
    for (int e = 0; e < 4; ++e) uu[4 * g + e] = U[(size_t)(trow + 8 * g + e) * D_ + dcol];
  }
#pragma unroll
  for (int s = 0; s < 6; ++s) stage(s, s);
  unsigned aadh[4], aadh2[4];
#pragma unroll
  for (int ks = 0; ks < 4; ++ks) { aadh[ks] = aad[ks] + 2 * B_STAGE; aadh2[ks] = aad[ks] + 4 * B_STAGE; }
  const unsigned badh = bad + 2 * B_STAGE, badh2 = bad + 4 * B_STAGE;
  bf16x8 a[2][4]; s16x4 bl[2][4], bh[2][4];
#define B_RD2(S, ks, AA, BB, bo) DS_B128(a[S][ks], AA[ks], bo); DS_TR(bl[S][ks], BB, bo + ks * 2048); DS_TR(bh[S][ks], BB, bo + ks * 2048 + 512);
#define B_RD(S, ks, buf) if constexpr ((buf) < 2) { B_RD2(S, ks, aad, bad, (buf) * B_STAGE) } else if constexpr ((buf) < 4) { B_RD2(S, ks, aadh, badh, ((buf) - 2) * B_STAGE) } else { B_RD2(S, ks, aadh2, badh2, ((buf) - 4) * B_STAGE) }
#define B_MM(S, ks) { acc = MFMA_BF(a[S][ks], cat_bf(bl[S][ks], bh[S][ks]), acc); }
#define B_STEP(kt, buf, S, WAITN) { \
    asm volatile("s_waitcnt vmcnt(" #WAITN ") lgkmcnt(0)" ::: "memory"); \
    __builtin_amdgcn_s_barrier(); \
 \
    if ((kt) + 6 < 16) stage((kt) + 6, buf); \
    if ((kt) < 15) { B_RD((S) ^ 1, 0, ((buf) + 1) % 6) B_RD((S) ^ 1, 1, ((buf) + 1) % 6) } \
    SCHED(); B_MM(S, 0) B_MM(S, 1) SCHED(); \
    if ((kt) < 15) { B_RD((S) ^ 1, 2, ((buf) + 1) % 6) B_RD((S) ^ 1, 3, ((buf) + 1) % 6) } \
    SCHED(); B_MM(S, 2) B_MM(S, 3) asm volatile("" : "+v"(acc)); SCHED(); }
  asm volatile("s_waitcnt vmcnt(15)" ::: "memory");
  __builtin_amdgcn_s_barrier();
  B_RD(0, 0, 0) B_RD(0, 1, 0) B_RD(0, 2, 0) B_RD(0, 3, 0)
#pragma unroll 1
  for (int kt = 0; kt < 12; kt += 6) {
    B_STEP(kt, 0, 0, 12) B_STEP(kt + 1, 1, 1, 12) B_STEP(kt + 2, 2, 0, 12) B_STEP(kt + 3, 3, 1, 12) B_STEP(kt + 4, 4, 0, 12)
    if (kt != 0) asm volatile("s_waitcnt vmcnt(9)" ::: "memory");
    B_STEP(kt + 5, 5, 1, 12)
  }
  B_STEP(12, 0, 0, 6) B_STEP(13, 1, 1, 3) B_STEP(14, 2, 0, 0) B_STEP(15, 3, 1, 0)
#pragma unroll
  for (int g = 0; g < 4; ++g) {
#pragma unroll
    for (int e2 = 0; e2 < 2; ++e2) {
      unsigned ud = pack2bf(bf2f(uu[4 * g + 2 * e2]) * dsk, bf2f(uu[4 * g + 2 * e2 + 1]) * dsk);
      unsigned a = pack2bf(lo_f(yv[g][e2]) + lo_f(ud), hi_f(yv[g][e2]) + hi_f(ud));
      unsigned bb = pack2bf(acc[4 * g + 2 * e2], acc[4 * g + 2 * e2 + 1]);
      float g0 = gv[g][2 * e2], g1 = gv[g][2 * e2 + 1];
      unsigned og = pack2bf(1.0f - g0, 1.0f - g1);
      unsigned ga_ = pack2bf(g0 * lo_f(a), g1 * hi_f(a));
      unsigned gb_ = pack2bf(lo_f(og) * lo_f(bb), hi_f(og) * hi_f(bb));
      unsigned r = pack2bf(lo_f(ga_) + lo_f(gb_), hi_f(ga_) + hi_f(gb_));
      int t = trow + 8 * g + 2 * e2;
      out[(size_t)t * D_ + dcol] = (u16)(r & 0xffffu);
      out[(size_t)(t + 1) * D_ + dcol] = (u16)(r >> 16);
    }
  }

}

extern "C" void kernel_launch(void* const* d_in, const int* in_sizes, int n_in, void* d_out, int out_size, void* d_ws,
                              size_t ws_size, hipStream_t stream) {
  const u16* U = (const u16*)d_in[0];
  char* ws = (char*)d_ws;
  (void)hipFuncSetAttribute((const void*)kernelA, hipFuncAttributeMaxDynamicSharedMemorySize, A_LDS);
  (void)hipFuncSetAttribute((const void*)kernelM, hipFuncAttributeMaxDynamicSharedMemorySize, CV_LDS);
  (void)hipFuncSetAttribute((const void*)kernelB, hipFuncAttributeMaxDynamicSharedMemorySize, B_STAGE * B_NST);
  kernelA<<<512, 512, A_LDS, stream>>>(U, (const u16*)d_in[1], (const u16*)d_in[2], (const u16*)d_in[3],
                                       (const u16*)d_in[4], (const u16*)d_in[5], (const u16*)d_in[6],
                                       (const u16*)d_in[7], (const u16*)d_in[10], ws);
  kernelM<<<512 + 256 + 128, 256, CV_LDS, stream>>>(U, (const u16*)d_in[12], (const u16*)d_in[11], ws);
  kernelB<<<256, 512, B_STAGE * B_NST, stream>>>(U, (const u16*)d_in[8], (const u16*)d_in[11], ws, (u16*)d_out);
}
```
